```python
import torch

def _softmax(x, axis=-1):
    return torch.softmax(x, dim=axis)

def _gelu(x):
    return torch.nn.functional.gelu(x, approximate='tanh')

def _silu(x):
    return torch.nn.functional.silu(x)

def _sigmoid(x):
    return torch.sigmoid(x)

def _layernorm(x, axis=-1, eps=1e-05):
    m = x.mean(dim=axis, keepdim=True)
    v = x.var(dim=axis, keepdim=True, unbiased=False)
    return (x - m) / torch.sqrt(v + eps)

def _scatter_add(tgt, idx, src, axis=0):
    return tgt.clone().index_add_(axis, idx, src)

def _segment_sum(src, seg, n):
    out = torch.zeros((n,) + src.shape[1:], dtype=src.dtype)
    return out.index_add_(0, seg, src)

def _pad(x, pw):
    flat = tuple((v for pair in reversed(pw) for v in pair))
    return torch.nn.functional.pad(x, flat)
kernel_name = 'adaLN_cond__gqa_block_cached__glu_gated_geglu__prompt'

def setup_inputs():

    _w = lambda a: (lambda t: t.to(torch.bfloat16) if t.is_floating_point() else t)(torch.as_tensor(a))

    def _A_setup():
        B, Sq, D = (1, 2048, 512)
        Hq, Hkv, Sk, Dh = (32, 8, 2048, 128)
        assert Hq % Hkv == 0, (Hq, Hkv)
        return {'x': _w((lambda *s: torch.randn(*s, dtype=torch.bfloat16) * 0.05)(B, Sq, D)), 'k_cache': _w((lambda *s: torch.randn(*s, dtype=torch.bfloat16) * 0.8)(B, Hkv, Sk, Dh)), 'v_cache': _w((lambda *s: torch.randn(*s, dtype=torch.bfloat16) * 3.2)(B, Hkv, Sk, Dh)), 'wq': _w((lambda *s: torch.randn(*s, dtype=torch.bfloat16) * 0.8)(D, Hq * Dh) * D ** (-0.5)), 'wo': _w((lambda *s: torch.randn(*s, dtype=torch.bfloat16) * 3.2)(Hq * Dh, D) * (Hq * Dh) ** (-0.5))}
    _A_in = _A_setup()

    def _B_setup():
        B, S, Dm, Df = (1, 2048, 512, 1792)
        return {'x': _w((lambda *s: torch.randn(*s, dtype=torch.bfloat16) * 0.05)(B, S, Dm)), 'w_gate': _w((lambda *s: torch.randn(*s, dtype=torch.bfloat16) * 3.2)(Dm, Df) * Dm ** (-0.5)), 'w_up': _w((lambda *s: torch.randn(*s, dtype=torch.bfloat16) * 3.2)(Dm, Df) * Dm ** (-0.5)), 'w_down': _w((lambda *s: torch.randn(*s, dtype=torch.bfloat16) * 3.2)(Df, Dm) * Df ** (-0.5))}
    _B_in = _B_setup()
    return {'A_x': _A_in['x'], 'A_k_cache': _A_in['k_cache'], 'A_v_cache': _A_in['v_cache'], 'A_wq': _A_in['wq'], 'A_wo': _A_in['wo'], 'B_x': _B_in['x'], 'B_w_gate': _B_in['w_gate'], 'B_w_up': _B_in['w_up'], 'B_w_down': _B_in['w_down'], 'c': _w((lambda *s: torch.randn(*s, dtype=torch.bfloat16) * 0.4)(1, 512) * 8.0), 'w_mod': _w((lambda *s: torch.randn(*s, dtype=torch.bfloat16) * 0.4)(512, 6 * 512) * 512 ** (-0.5) * 8.0)}

def reference(A_x, A_k_cache, A_v_cache, A_wq, A_wo, B_x, B_w_gate, B_w_up, B_w_down, c, w_mod):

    def _A_ref(x, k_cache, v_cache, wq, wo):
        B, Sq, D = x.shape
        Hq, Dh = (32, 128)
        Hkv = k_cache.shape[1]
        rep = Hq // Hkv
        q = (lambda x, *d: x.permute(*d))((x @ wq).reshape(B, Sq, Hq, Dh), 0, 2, 1, 3)
        k = (lambda x, n, axis: torch.repeat_interleave(x, n, dim=axis))(k_cache, rep, axis=1)
        v = (lambda x, n, axis: torch.repeat_interleave(x, n, dim=axis))(v_cache, rep, axis=1)
        scores = torch.einsum('bhqd,bhkd->bhqk', q, k) * Dh ** (-0.5)
        p = _softmax(scores, axis=-1)
        o = torch.einsum('bhqk,bhkd->bhqd', p, v)
        return (lambda x, *d: x.permute(*d))(o, 0, 2, 1, 3).reshape(B, Sq, Hq * Dh) @ wo

    def _B_ref(x, w_gate, w_up, w_down):
        g = torch.einsum('bsd,df->bsf', x, w_gate)
        u = torch.einsum('bsd,df->bsf', x, w_up)
        h = _gelu(g) * u
        return torch.einsum('bsf,fd->bsd', h, w_down)
    x = A_x
    mod = (_silu(c) @ w_mod).reshape(c.shape[0], 6, 1, -1)
    sa, ca, ga, sb, cb, gb = (mod[:, 0].clone(), mod[:, 1].clone(), mod[:, 2].clone(), mod[:, 3].clone(), mod[:, 4].clone(), mod[:, 5].clone())
    h = _layernorm(x, axis=-1) * (1 + ca) + sa
    x = x + ga * _A_ref(h, A_k_cache, A_v_cache, A_wq, A_wo)
    h = _layernorm(x, axis=-1) * (1 + cb) + sb
    x = x + gb * _B_ref(h, B_w_gate, B_w_up, B_w_down)
    return x
```

```cpp
#include <hip/hip_runtime.h>
#include <utility>

constexpr int SEQ = 2048, DM = 512, HQ = 32, HKV = 8, DH = 128, DFF = 1792;
constexpr int KVBLK = 64, NT = SEQ / KVBLK;
#define THRV 30.0f
constexpr float THR = THRV;
constexpr unsigned KRS = 256u, QRS = HQ * 256u, KTILEB = 64u * KRS;
constexpr size_t SHM_ATTN = 163840;
typedef float    f32x16 __attribute__((ext_vector_type(16)));
typedef float    f32x2  __attribute__((ext_vector_type(2)));
typedef float    f32x4  __attribute__((ext_vector_type(4)));
typedef __bf16   bf16x8 __attribute__((ext_vector_type(8)));
typedef __bf16   bf16x4 __attribute__((ext_vector_type(4)));
typedef __bf16   bf16x2 __attribute__((ext_vector_type(2)));
typedef unsigned u32x4  __attribute__((ext_vector_type(4)));
typedef unsigned u32x2  __attribute__((ext_vector_type(2)));
template <int... Is> using iseq = std::integer_sequence<int, Is...>;
template <int N> using make_iseq = std::make_integer_sequence<int, N>;

#define KRING 0x0000u
#define VRING 0x8000u
#define QSTAGE 0x10000u
#define OSTAGE 0x20000u
#define SRD_WORD3 0x00020000
#define DI __device__ __forceinline__
#define GAP() __builtin_amdgcn_sched_barrier(0)
#define W4_ACLOB_HI "a128","a129","a130","a131","a132","a133","a134","a135","a136","a137","a138","a139","a140","a141","a142","a143","a144","a145","a146","a147","a148","a149","a150","a151","a152","a153","a154","a155","a156","a157","a158","a159","a160","a161","a162","a163","a164","a165","a166","a167","a168","a169","a170","a171","a172","a173","a174","a175","a176","a177","a178","a179","a180","a181","a182","a183","a184","a185","a186","a187","a188","a189","a190","a191","a192","a193","a194","a195","a196","a197","a198","a199","a200","a201","a202","a203","a204","a205","a206","a207","a208","a209","a210","a211","a212","a213","a214","a215","a216","a217","a218","a219","a220","a221","a222","a223","a224","a225","a226","a227","a228","a229","a230","a231","a232","a233","a234","a235","a236","a237","a238","a239","a240","a241","a242","a243","a244","a245","a246","a247","a248","a249","a250","a251","a252","a253","a254","a255"
#define W4_ACLOB "a0","a1","a2","a3","a4","a5","a6","a7","a8","a9","a10","a11","a12","a13","a14","a15","a16","a17","a18","a19","a20","a21","a22","a23","a24","a25","a26","a27","a28","a29","a30","a31","a32","a33","a34","a35","a36","a37","a38","a39","a40","a41","a42","a43","a44","a45","a46","a47","a48","a49","a50","a51","a52","a53","a54","a55","a56","a57","a58","a59","a60","a61","a62","a63","a64","a65","a66","a67","a68","a69","a70","a71","a72","a73","a74","a75","a76","a77","a78","a79","a80","a81","a82","a83","a84","a85","a86","a87","a88","a89","a90","a91","a92","a93","a94","a95","a96","a97","a98","a99","a100","a101","a102","a103","a104","a105","a106","a107","a108","a109","a110","a111","a112","a113","a114","a115","a116","a117","a118","a119","a120","a121","a122","a123","a124","a125","a126","a127"
#define W4_READ_O(DB, QB, o) do { switch (2 * (DB) + (QB)) { \
    case 0: asm volatile("v_accvgpr_read_b32 %0, a0\n\t" "v_accvgpr_read_b32 %1, a1\n\t" "v_accvgpr_read_b32 %2, a2\n\t" "v_accvgpr_read_b32 %3, a3\n\t" "v_accvgpr_read_b32 %4, a4\n\t" "v_accvgpr_read_b32 %5, a5\n\t" "v_accvgpr_read_b32 %6, a6\n\t" "v_accvgpr_read_b32 %7, a7\n\t" "v_accvgpr_read_b32 %8, a8\n\t" "v_accvgpr_read_b32 %9, a9\n\t" "v_accvgpr_read_b32 %10, a10\n\t" "v_accvgpr_read_b32 %11, a11\n\t" "v_accvgpr_read_b32 %12, a12\n\t" "v_accvgpr_read_b32 %13, a13\n\t" "v_accvgpr_read_b32 %14, a14\n\t" "v_accvgpr_read_b32 %15, a15\n\t" : "=v"((o)[0]), "=v"((o)[1]), "=v"((o)[2]), "=v"((o)[3]), "=v"((o)[4]), "=v"((o)[5]), "=v"((o)[6]), "=v"((o)[7]), "=v"((o)[8]), "=v"((o)[9]), "=v"((o)[10]), "=v"((o)[11]), "=v"((o)[12]), "=v"((o)[13]), "=v"((o)[14]), "=v"((o)[15])); break; \
    case 1: asm volatile("v_accvgpr_read_b32 %0, a16\n\t" "v_accvgpr_read_b32 %1, a17\n\t" "v_accvgpr_read_b32 %2, a18\n\t" "v_accvgpr_read_b32 %3, a19\n\t" "v_accvgpr_read_b32 %4, a20\n\t" "v_accvgpr_read_b32 %5, a21\n\t" "v_accvgpr_read_b32 %6, a22\n\t" "v_accvgpr_read_b32 %7, a23\n\t" "v_accvgpr_read_b32 %8, a24\n\t" "v_accvgpr_read_b32 %9, a25\n\t" "v_accvgpr_read_b32 %10, a26\n\t" "v_accvgpr_read_b32 %11, a27\n\t" "v_accvgpr_read_b32 %12, a28\n\t" "v_accvgpr_read_b32 %13, a29\n\t" "v_accvgpr_read_b32 %14, a30\n\t" "v_accvgpr_read_b32 %15, a31\n\t" : "=v"((o)[0]), "=v"((o)[1]), "=v"((o)[2]), "=v"((o)[3]), "=v"((o)[4]), "=v"((o)[5]), "=v"((o)[6]), "=v"((o)[7]), "=v"((o)[8]), "=v"((o)[9]), "=v"((o)[10]), "=v"((o)[11]), "=v"((o)[12]), "=v"((o)[13]), "=v"((o)[14]), "=v"((o)[15])); break; \
    case 2: asm volatile("v_accvgpr_read_b32 %0, a32\n\t" "v_accvgpr_read_b32 %1, a33\n\t" "v_accvgpr_read_b32 %2, a34\n\t" "v_accvgpr_read_b32 %3, a35\n\t" "v_accvgpr_read_b32 %4, a36\n\t" "v_accvgpr_read_b32 %5, a37\n\t" "v_accvgpr_read_b32 %6, a38\n\t" "v_accvgpr_read_b32 %7, a39\n\t" "v_accvgpr_read_b32 %8, a40\n\t" "v_accvgpr_read_b32 %9, a41\n\t" "v_accvgpr_read_b32 %10, a42\n\t" "v_accvgpr_read_b32 %11, a43\n\t" "v_accvgpr_read_b32 %12, a44\n\t" "v_accvgpr_read_b32 %13, a45\n\t" "v_accvgpr_read_b32 %14, a46\n\t" "v_accvgpr_read_b32 %15, a47\n\t" : "=v"((o)[0]), "=v"((o)[1]), "=v"((o)[2]), "=v"((o)[3]), "=v"((o)[4]), "=v"((o)[5]), "=v"((o)[6]), "=v"((o)[7]), "=v"((o)[8]), "=v"((o)[9]), "=v"((o)[10]), "=v"((o)[11]), "=v"((o)[12]), "=v"((o)[13]), "=v"((o)[14]), "=v"((o)[15])); break; \
    case 3: asm volatile("v_accvgpr_read_b32 %0, a48\n\t" "v_accvgpr_read_b32 %1, a49\n\t" "v_accvgpr_read_b32 %2, a50\n\t" "v_accvgpr_read_b32 %3, a51\n\t" "v_accvgpr_read_b32 %4, a52\n\t" "v_accvgpr_read_b32 %5, a53\n\t" "v_accvgpr_read_b32 %6, a54\n\t" "v_accvgpr_read_b32 %7, a55\n\t" "v_accvgpr_read_b32 %8, a56\n\t" "v_accvgpr_read_b32 %9, a57\n\t" "v_accvgpr_read_b32 %10, a58\n\t" "v_accvgpr_read_b32 %11, a59\n\t" "v_accvgpr_read_b32 %12, a60\n\t" "v_accvgpr_read_b32 %13, a61\n\t" "v_accvgpr_read_b32 %14, a62\n\t" "v_accvgpr_read_b32 %15, a63\n\t" : "=v"((o)[0]), "=v"((o)[1]), "=v"((o)[2]), "=v"((o)[3]), "=v"((o)[4]), "=v"((o)[5]), "=v"((o)[6]), "=v"((o)[7]), "=v"((o)[8]), "=v"((o)[9]), "=v"((o)[10]), "=v"((o)[11]), "=v"((o)[12]), "=v"((o)[13]), "=v"((o)[14]), "=v"((o)[15])); break; \
    case 4: asm volatile("v_accvgpr_read_b32 %0, a64\n\t" "v_accvgpr_read_b32 %1, a65\n\t" "v_accvgpr_read_b32 %2, a66\n\t" "v_accvgpr_read_b32 %3, a67\n\t" "v_accvgpr_read_b32 %4, a68\n\t" "v_accvgpr_read_b32 %5, a69\n\t" "v_accvgpr_read_b32 %6, a70\n\t" "v_accvgpr_read_b32 %7, a71\n\t" "v_accvgpr_read_b32 %8, a72\n\t" "v_accvgpr_read_b32 %9, a73\n\t" "v_accvgpr_read_b32 %10, a74\n\t" "v_accvgpr_read_b32 %11, a75\n\t" "v_accvgpr_read_b32 %12, a76\n\t" "v_accvgpr_read_b32 %13, a77\n\t" "v_accvgpr_read_b32 %14, a78\n\t" "v_accvgpr_read_b32 %15, a79\n\t" : "=v"((o)[0]), "=v"((o)[1]), "=v"((o)[2]), "=v"((o)[3]), "=v"((o)[4]), "=v"((o)[5]), "=v"((o)[6]), "=v"((o)[7]), "=v"((o)[8]), "=v"((o)[9]), "=v"((o)[10]), "=v"((o)[11]), "=v"((o)[12]), "=v"((o)[13]), "=v"((o)[14]), "=v"((o)[15])); break; \
    case 5: asm volatile("v_accvgpr_read_b32 %0, a80\n\t" "v_accvgpr_read_b32 %1, a81\n\t" "v_accvgpr_read_b32 %2, a82\n\t" "v_accvgpr_read_b32 %3, a83\n\t" "v_accvgpr_read_b32 %4, a84\n\t" "v_accvgpr_read_b32 %5, a85\n\t" "v_accvgpr_read_b32 %6, a86\n\t" "v_accvgpr_read_b32 %7, a87\n\t" "v_accvgpr_read_b32 %8, a88\n\t" "v_accvgpr_read_b32 %9, a89\n\t" "v_accvgpr_read_b32 %10, a90\n\t" "v_accvgpr_read_b32 %11, a91\n\t" "v_accvgpr_read_b32 %12, a92\n\t" "v_accvgpr_read_b32 %13, a93\n\t" "v_accvgpr_read_b32 %14, a94\n\t" "v_accvgpr_read_b32 %15, a95\n\t" : "=v"((o)[0]), "=v"((o)[1]), "=v"((o)[2]), "=v"((o)[3]), "=v"((o)[4]), "=v"((o)[5]), "=v"((o)[6]), "=v"((o)[7]), "=v"((o)[8]), "=v"((o)[9]), "=v"((o)[10]), "=v"((o)[11]), "=v"((o)[12]), "=v"((o)[13]), "=v"((o)[14]), "=v"((o)[15])); break; \
    case 6: asm volatile("v_accvgpr_read_b32 %0, a96\n\t" "v_accvgpr_read_b32 %1, a97\n\t" "v_accvgpr_read_b32 %2, a98\n\t" "v_accvgpr_read_b32 %3, a99\n\t" "v_accvgpr_read_b32 %4, a100\n\t" "v_accvgpr_read_b32 %5, a101\n\t" "v_accvgpr_read_b32 %6, a102\n\t" "v_accvgpr_read_b32 %7, a103\n\t" "v_accvgpr_read_b32 %8, a104\n\t" "v_accvgpr_read_b32 %9, a105\n\t" "v_accvgpr_read_b32 %10, a106\n\t" "v_accvgpr_read_b32 %11, a107\n\t" "v_accvgpr_read_b32 %12, a108\n\t" "v_accvgpr_read_b32 %13, a109\n\t" "v_accvgpr_read_b32 %14, a110\n\t" "v_accvgpr_read_b32 %15, a111\n\t" : "=v"((o)[0]), "=v"((o)[1]), "=v"((o)[2]), "=v"((o)[3]), "=v"((o)[4]), "=v"((o)[5]), "=v"((o)[6]), "=v"((o)[7]), "=v"((o)[8]), "=v"((o)[9]), "=v"((o)[10]), "=v"((o)[11]), "=v"((o)[12]), "=v"((o)[13]), "=v"((o)[14]), "=v"((o)[15])); break; \
    case 7: asm volatile("v_accvgpr_read_b32 %0, a112\n\t" "v_accvgpr_read_b32 %1, a113\n\t" "v_accvgpr_read_b32 %2, a114\n\t" "v_accvgpr_read_b32 %3, a115\n\t" "v_accvgpr_read_b32 %4, a116\n\t" "v_accvgpr_read_b32 %5, a117\n\t" "v_accvgpr_read_b32 %6, a118\n\t" "v_accvgpr_read_b32 %7, a119\n\t" "v_accvgpr_read_b32 %8, a120\n\t" "v_accvgpr_read_b32 %9, a121\n\t" "v_accvgpr_read_b32 %10, a122\n\t" "v_accvgpr_read_b32 %11, a123\n\t" "v_accvgpr_read_b32 %12, a124\n\t" "v_accvgpr_read_b32 %13, a125\n\t" "v_accvgpr_read_b32 %14, a126\n\t" "v_accvgpr_read_b32 %15, a127\n\t" : "=v"((o)[0]), "=v"((o)[1]), "=v"((o)[2]), "=v"((o)[3]), "=v"((o)[4]), "=v"((o)[5]), "=v"((o)[6]), "=v"((o)[7]), "=v"((o)[8]), "=v"((o)[9]), "=v"((o)[10]), "=v"((o)[11]), "=v"((o)[12]), "=v"((o)[13]), "=v"((o)[14]), "=v"((o)[15])); break; \
    } } while (0)

DI unsigned rfl(unsigned x) { return __builtin_amdgcn_readfirstlane(x); }
DI unsigned cvtpk(float lo, float hi) {
  f32x2 v = {lo, hi}; bf16x2 b = __builtin_convertvector(v, bf16x2); return __builtin_bit_cast(unsigned, b);
}
DI float max3f(float a, float b, float c) { float r; asm("v_max3_f32 %0, %1, %2, %3" : "=v"(r) : "v"(a), "v"(b), "v"(c)); return r; }
DI float max2f(float a, float b) { float r; asm("v_max_f32_e32 %0, %1, %2" : "=v"(r) : "v"(a), "v"(b)); return r; }
DI float fadd_s(float a, float b) { float r; asm("v_add_f32_e32 %0, %1, %2" : "=v"(r) : "v"(a), "v"(b)); return r; }
DI float fmul_s(float a, float b) { float r; asm("v_mul_f32_e32 %0, %1, %2" : "=v"(r) : "v"(a), "v"(b)); return r; }
DI float fsub_s(float a, float b) { float r; asm("v_sub_f32_e32 %0, %1, %2" : "=v"(r) : "v"(a), "v"(b)); return r; }
DI float w4_aone() { unsigned l; asm volatile("v_mbcnt_lo_u32_b32 %0, -1, 0\n\tv_mbcnt_hi_u32_b32 %0, -1, %0" : "=v"(l)); return l < 32u ? 1.f : 0.f; }
DI void negm_bcast(f32x16& negm, float aone, float neg_mhat) {
  asm volatile("s_nop 1\n\tv_mfma_f32_32x32x2_f32 %0, %1, %2, 0" : "+v"(negm) : "v"(aone), "v"(neg_mhat));
}
DI void negm_bcast_first(f32x16& negm, float aone, float neg_mhat) {
  asm volatile("s_nop 1\n\tv_mfma_f32_32x32x2_f32 %0, %1, %2, 0" : "=&v"(negm) : "v"(aone), "v"(neg_mhat));
}

struct W4C {
  unsigned vkb[4], vvb, vksrc[2], vvsrc, m0k, m0v, sofk, sofv, ldsb;
  __amdgpu_buffer_rsrc_t srdK, srdV;
  __amdgpu_buffer_rsrc_t srdKn, srdVn, srdQn; unsigned qsofn, qstepn, m0q;
  int T; float THR;
  f32x16 S[2][2][2];
  f32x16 NEGM[2];
  bf16x8 Vf[4][4];
  bf16x8 Pk[2][4];
  bf16x4 vlo[4][4], vup[4][4];
  float mhat[2], lsum[2], fct[2]; bool pend;
  float rs0[2], rs1[2], pmx0[2], pmx1[2], rmax[2], delta[2];
  unsigned pm0[8], psf[8];
};

constexpr int AO_(int db, int qb) { return 16 * (2 * db + qb); }
constexpr int AQ_(int qb, int d0) { return 128 + 32 * qb + 4 * d0; }
constexpr int AK_(int kb, int d0) { return 192 + 32 * kb + 4 * d0; }

template <int I> DI void tr_read(W4C& c, unsigned vslot) {
  constexpr int ks = I >> 3, db = (I >> 1) & 3;
  if constexpr ((I & 1) == 0) asm volatile("ds_read_b64_tr_b16 %0, %1 offset:%2" : "=v"(c.vlo[db][ks]) : "v"(c.vvb + vslot), "i"(4096 * ks + 512 * db) : "memory");
  else                        asm volatile("ds_read_b64_tr_b16 %0, %1 offset:%2" : "=v"(c.vup[db][ks]) : "v"(c.vvb + vslot), "i"(4096 * ks + 2048 + 512 * db) : "memory");
}
template <int I> DI void k_read(W4C& c, unsigned slot) {
  constexpr int kb = I >> 3, d0 = I & 7;
  asm volatile("ds_read_b128 a[%c1:%c2], %0 offset:%c3" :: "v"(c.vkb[d0 & 3] + slot), "i"(AK_(kb, d0)), "i"(AK_(kb, d0) + 3), "i"(8192 * kb + 128 * (d0 >> 2)) : "memory", W4_ACLOB_HI);
}
template <int I> DI void q_read(W4C& c, unsigned qoff) {
  constexpr int qb = I >> 3, d0 = I & 7;
  asm volatile("ds_read_b128 a[%c1:%c2], %0 offset:%c3" :: "v"(c.vkb[d0 & 3] + qoff), "i"(AQ_(qb, d0)), "i"(AQ_(qb, d0) + 3), "i"(8192 * qb + 128 * (d0 >> 2)) : "memory", W4_ACLOB_HI);
}
DI bf16x8 q_ldread(unsigned base, const int qb, const int d0) {
  bf16x8 r;
  switch (2 * qb + (d0 >> 2)) {
    case 0: asm volatile("ds_read_b128 %0, %1 offset:0"    : "=v"(r) : "v"(base) : "memory"); break;
    case 1: asm volatile("ds_read_b128 %0, %1 offset:128"  : "=v"(r) : "v"(base) : "memory"); break;
    case 2: asm volatile("ds_read_b128 %0, %1 offset:8192" : "=v"(r) : "v"(base) : "memory"); break;
    default: asm volatile("ds_read_b128 %0, %1 offset:8320" : "=v"(r) : "v"(base) : "memory"); break;
  }
  return r;
}
template <int I> DI void q_write1(const u32x4& w) {
  constexpr int aq = AQ_(I >> 3, I & 7);
  asm volatile("v_accvgpr_write_b32 a[%c4], %0\n\tv_accvgpr_write_b32 a[%c5], %1\n\tv_accvgpr_write_b32 a[%c6], %2\n\tv_accvgpr_write_b32 a[%c7], %3"
               :: "v"(w[0]), "v"(w[1]), "v"(w[2]), "v"(w[3]), "i"(aq), "i"(aq + 1), "i"(aq + 2), "i"(aq + 3) : W4_ACLOB_HI);
}
DI void q_write(const int qb, const int d0, const u32x4& w) {
  switch (8 * qb + d0) {
    case 0: q_write1<0>(w); break;   case 1: q_write1<1>(w); break;   case 2: q_write1<2>(w); break;   case 3: q_write1<3>(w); break;
    case 4: q_write1<4>(w); break;   case 5: q_write1<5>(w); break;   case 6: q_write1<6>(w); break;   case 7: q_write1<7>(w); break;
    case 8: q_write1<8>(w); break;   case 9: q_write1<9>(w); break;   case 10: q_write1<10>(w); break; case 11: q_write1<11>(w); break;
    case 12: q_write1<12>(w); break; case 13: q_write1<13>(w); break; case 14: q_write1<14>(w); break; case 15: q_write1<15>(w); break;
  }
}
template <int X, bool FIRST, int M> DI void qk_mfma(W4C& c) {
  constexpr int d0 = M >> 2, kb = (M >> 1) & 1, qb = M & 1, ak = AK_(kb, d0), aq = AQ_(qb, d0);
  if constexpr (d0 == 0) {
    if constexpr (FIRST) asm volatile("v_mfma_f32_32x32x16_bf16 %0, a[%c1:%c2], a[%c3:%c4], 0" : "=&v"(c.S[X][kb][qb]) : "i"(ak), "i"(ak + 3), "i"(aq), "i"(aq + 3) : W4_ACLOB, W4_ACLOB_HI);
    else                 asm volatile("v_mfma_f32_32x32x16_bf16 %0, a[%c2:%c3], a[%c4:%c5], %1" : "=&v"(c.S[X][kb][qb]) : "v"(c.NEGM[qb]), "i"(ak), "i"(ak + 3), "i"(aq), "i"(aq + 3) : W4_ACLOB, W4_ACLOB_HI);
  } else asm volatile("v_mfma_f32_32x32x16_bf16 %0, a[%c1:%c2], a[%c3:%c4], %0" : "+v"(c.S[X][kb][qb]) : "i"(ak), "i"(ak + 3), "i"(aq), "i"(aq + 3) : W4_ACLOB, W4_ACLOB_HI);
}
template <int M, bool ZERO> DI void pv_mfma(W4C& c) {
  constexpr int ks = M >> 3, db = (M >> 1) & 3, qb = M & 1, ao = AO_(db, qb);
  if constexpr (ZERO && ks == 0) asm volatile("v_mfma_f32_32x32x16_bf16 a[%c2:%c3], %0, %1, 0" :: "v"(c.Vf[db][ks]), "v"(c.Pk[qb][ks]), "i"(ao), "i"(ao + 15) : W4_ACLOB);
  else                         asm volatile("v_mfma_f32_32x32x16_bf16 a[%c2:%c3], %0, %1, a[%c2:%c3]" :: "v"(c.Vf[db][ks]), "v"(c.Pk[qb][ks]), "i"(ao), "i"(ao + 15) : W4_ACLOB);
}
template <int X, int QB, int KS, int P> DI void pack1(W4C& c) {
  u32x4 w = __builtin_bit_cast(u32x4, c.Pk[QB][KS]);
  w[P] = cvtpk(c.S[X][KS >> 1][QB][8 * (KS & 1) + 2 * P], c.S[X][KS >> 1][QB][8 * (KS & 1) + 2 * P + 1]);
  c.Pk[QB][KS] = __builtin_bit_cast(bf16x8, w);
}
template <int X, int J> DI void rowsum1(W4C& c) {
  constexpr int qb = J >> 4, ks = (J >> 2) & 3, p = J & 3;
  c.rs0[qb] = fadd_s(c.rs0[qb], c.S[X][ks >> 1][qb][8 * (ks & 1) + 2 * p]); c.rs1[qb] = fadd_s(c.rs1[qb], c.S[X][ks >> 1][qb][8 * (ks & 1) + 2 * p + 1]);
}

template <int X, bool FIRST, int M, bool NEXTK = false, bool NEXTV = false> DI void gapA(W4C& c, unsigned vslot, const int t) {
  constexpr int Xp = X ^ 1;
  qk_mfma<X, FIRST, M>(c);
  if constexpr (M >= 16 && M <= 31) {
    const int tk = NEXTK ? t + 2 - c.T : t + 2, tv = NEXTV ? t + 1 - c.T : t + 1;
    constexpr int q = (M - 16) >> 1;
    if constexpr (((M - 16) & 1) == 0) {
      if constexpr (q < 4) c.pm0[q] = c.m0k + (unsigned)((t + 2) & 1) * 0x4000u + 1024u * q; else c.pm0[q] = c.m0v + (unsigned)((t + 1) & 1) * 0x4000u + 1024u * (q - 4);
      asm volatile("" : "+s"(c.pm0[q]));
    } else {
      if constexpr (q < 4) c.psf[q] = KTILEB * (unsigned)tk + c.sofk + KRS * 4u * q; else c.psf[q] = KTILEB * (unsigned)tv + c.sofv + KRS * 8u * ((q - 4) >> 1) + 128u * ((q - 4) & 1);
      asm volatile("" : "+s"(c.psf[q]));
    }
  }
  if constexpr (M == 31) asm volatile("s_nop 4" : "+s"(c.pm0[0]), "+s"(c.pm0[1]), "+s"(c.pm0[2]), "+s"(c.pm0[3]), "+s"(c.pm0[4]), "+s"(c.pm0[5]), "+s"(c.pm0[6]), "+s"(c.pm0[7]),
                                                  "+s"(c.psf[0]), "+s"(c.psf[1]), "+s"(c.psf[2]), "+s"(c.psf[3]), "+s"(c.psf[4]), "+s"(c.psf[5]), "+s"(c.psf[6]), "+s"(c.psf[7]));
  if constexpr (!FIRST && M == 8) asm volatile("" :: "v"(c.NEGM[0]), "v"(c.NEGM[1]));
  if constexpr (!FIRST) {
    constexpr int base = M + (M >= 4) + (M >= 8) + (M >= 12) + (M >= 16) + (M >= 20) + (M >= 24);
    if constexpr (M <= 25) tr_read<base>(c, vslot);
    if constexpr (M == 3 || M == 7 || M == 11 || M == 15 || M == 19 || M == 23) tr_read<base + 1>(c, vslot);
    if constexpr (M < 16) {
      constexpr int e0 = 2 * M, e1 = 2 * M + 1;
      c.S[Xp][1][e0 >> 4][e0 & 15] = __builtin_amdgcn_exp2f(c.S[Xp][1][e0 >> 4][e0 & 15]);
      c.S[Xp][1][e1 >> 4][e1 & 15] = __builtin_amdgcn_exp2f(c.S[Xp][1][e1 >> 4][e1 & 15]);
      pack1<Xp, (M >> 3), ((M >> 2) & 1), (M & 3)>(c);
    } else {
      constexpr int cc = M - 16;
      pack1<Xp, (cc >> 3), 2 + ((cc >> 2) & 1), (cc & 3)>(c);
      rowsum1<Xp, 16 * (cc >> 3) + 8 + 4 * ((cc >> 2) & 1) + (cc & 3)>(c);
    }
  }
  GAP();
}
template <int X, bool FIRST, bool NEXTK, bool NEXTV, int VMW, int... Ms> DI void phaseA_(W4C& c, const int t, iseq<Ms...>) {
  const unsigned vslot = (unsigned)((t - 1) & 1) * 0x4000u;
  (gapA<X, FIRST, Ms, NEXTK, NEXTV>(c, vslot, t), ...);
  if constexpr (!FIRST) {
    #pragma unroll
    for (int qb = 0; qb < 2; ++qb) {
      const float rs = fadd_s(c.rs0[qb], c.rs1[qb]);
      auto pr = __builtin_amdgcn_permlane32_swap(__float_as_uint(rs), __float_as_uint(rs), false, false);
      c.lsum[qb] = fadd_s(c.lsum[qb], fadd_s(__uint_as_float(pr[0]), __uint_as_float(pr[1])));
    }
  }
#define W4_VLO "+v"(c.vlo[0][0]), "+v"(c.vlo[0][1]), "+v"(c.vlo[0][2]), "+v"(c.vlo[0][3]), "+v"(c.vlo[1][0]), "+v"(c.vlo[1][1]), "+v"(c.vlo[1][2]), "+v"(c.vlo[1][3]), \
               "+v"(c.vlo[2][0]), "+v"(c.vlo[2][1]), "+v"(c.vlo[2][2]), "+v"(c.vlo[2][3]), "+v"(c.vlo[3][0]), "+v"(c.vlo[3][1]), "+v"(c.vlo[3][2]), "+v"(c.vlo[3][3])
#define W4_VUP "+v"(c.vup[0][0]), "+v"(c.vup[0][1]), "+v"(c.vup[0][2]), "+v"(c.vup[0][3]), "+v"(c.vup[1][0]), "+v"(c.vup[1][1]), "+v"(c.vup[1][2]), "+v"(c.vup[1][3]), \
               "+v"(c.vup[2][0]), "+v"(c.vup[2][1]), "+v"(c.vup[2][2]), "+v"(c.vup[2][3]), "+v"(c.vup[3][0]), "+v"(c.vup[3][1]), "+v"(c.vup[3][2]), "+v"(c.vup[3][3])
  if constexpr (FIRST) asm volatile("s_waitcnt vmcnt(0) lgkmcnt(0)\n\ts_barrier" ::: "memory");
  else { asm volatile("s_waitcnt vmcnt(%c16) lgkmcnt(0)" : W4_VLO : "i"(VMW) : "memory"); asm volatile("s_barrier" : W4_VUP :: "memory"); }
  GAP();
  if constexpr (!FIRST) {
    #pragma unroll
    for (int ks = 0; ks < 4; ++ks)
      #pragma unroll
      for (int db = 0; db < 4; ++db) c.Vf[db][ks] = __builtin_shufflevector(c.vlo[db][ks], c.vup[db][ks], 0, 1, 2, 3, 4, 5, 6, 7);
  }
}
template <int X, bool FIRST, bool NEXTK = false, bool NEXTV = false, int VMW = 0> DI void phaseA(W4C& c, const int t) { phaseA_<X, FIRST, NEXTK, NEXTV, VMW>(c, t, make_iseq<32>{}); }

template <int X, bool PEEL, bool ZERO1, int DK, int DV, bool KR, int M> DI void gapB(W4C& c, const unsigned kslot_rd) {
  if constexpr (!PEEL) pv_mfma<M, ZERO1>(c);
  if constexpr (M < 4 && DK == 1)             asm volatile("s_mov_b32 m0, %0\n\ts_nop 0\n\tbuffer_load_dwordx4 %1, %2, %3 offen lds" :: "s"(c.pm0[M]), "v"(c.vksrc[M & 1]), "s"(c.srdK),  "s"(c.psf[M]) : "m0", "memory");
  else if constexpr (M < 4 && DK == 2)        asm volatile("s_mov_b32 m0, %0\n\ts_nop 0\n\tbuffer_load_dwordx4 %1, %2, %3 offen lds" :: "s"(c.pm0[M]), "v"(c.vksrc[M & 1]), "s"(c.srdKn), "s"(c.psf[M]) : "m0", "memory");
  else if constexpr (M >= 4 && M < 8 && DV == 1) asm volatile("s_mov_b32 m0, %0\n\ts_nop 0\n\tbuffer_load_dwordx4 %1, %2, %3 offen lds" :: "s"(c.pm0[M]), "v"(c.vvsrc), "s"(c.srdV),  "s"(c.psf[M]) : "m0", "memory");
  else if constexpr (M >= 4 && M < 8 && DV == 2) asm volatile("s_mov_b32 m0, %0\n\ts_nop 0\n\tbuffer_load_dwordx4 %1, %2, %3 offen lds" :: "s"(c.pm0[M]), "v"(c.vvsrc), "s"(c.srdVn), "s"(c.psf[M]) : "m0", "memory");
  if constexpr (M >= 1 && M <= 16 && KR) k_read<M - 1>(c, kslot_rd);
  if constexpr (M >= 8 && M < 16) {
    constexpr int i = M - 8, q = i >> 2, r = 4 * (i & 3);
    if constexpr ((i & 3) == 0) { c.pmx0[q] = max3f(c.S[X][0][q][0], c.S[X][0][q][1], c.S[X][1][q][0]); c.pmx1[q] = max3f(c.S[X][0][q][2], c.S[X][0][q][3], c.S[X][1][q][1]);
                                  c.pmx0[q] = max3f(c.pmx0[q], c.S[X][1][q][2], c.S[X][1][q][3]); }
    else                        { c.pmx0[q] = max3f(c.pmx0[q], c.S[X][0][q][r], c.S[X][0][q][r + 1]); c.pmx1[q] = max3f(c.pmx1[q], c.S[X][0][q][r + 2], c.S[X][0][q][r + 3]);
                                  c.pmx0[q] = max3f(c.pmx0[q], c.S[X][1][q][r], c.S[X][1][q][r + 1]); c.pmx1[q] = max3f(c.pmx1[q], c.S[X][1][q][r + 2], c.S[X][1][q][r + 3]); }
  }
  if constexpr (M == 16 || M == 17) {
    constexpr int q = M - 16; const float mm = max2f(c.pmx0[q], c.pmx1[q]);
    auto pr = __builtin_amdgcn_permlane32_swap(__float_as_uint(mm), __float_as_uint(mm), false, false);
    c.rmax[q] = max2f(__uint_as_float(pr[0]), __uint_as_float(pr[1]));
  }
  if constexpr (M == 18) {
    if constexpr (PEEL) {
      #pragma unroll
      for (int q = 0; q < 2; ++q) { c.mhat[q] = c.rmax[q]; c.lsum[q] = 0.f; c.fct[q] = 1.f;
        #pragma unroll
        for (int kb = 0; kb < 2; ++kb) { _Pragma("unroll") for (int e = 0; e < 16; ++e) c.S[X][kb][q][e] = fsub_s(c.S[X][kb][q][e], c.rmax[q]); }
        negm_bcast_first(c.NEGM[q], w4_aone(), -c.mhat[q]);
      }
    } else {
      const bool grew = (c.rmax[0] > c.THR) | (c.rmax[1] > c.THR);
      if (__builtin_expect(__any((int)grew), 0)) {
        #pragma unroll
        for (int q = 0; q < 2; ++q) { const float dl = max2f(c.rmax[q], 0.f); c.delta[q] = dl; c.mhat[q] = fadd_s(c.mhat[q], dl); c.fct[q] = __builtin_amdgcn_exp2f(-dl);
          #pragma unroll
          for (int kb = 0; kb < 2; ++kb) { _Pragma("unroll") for (int e = 0; e < 16; ++e) c.S[X][kb][q][e] = fsub_s(c.S[X][kb][q][e], dl); }
          negm_bcast(c.NEGM[q], w4_aone(), -c.mhat[q]);
        }
        c.pend = true;
      }
    }
  }
  if constexpr (M == 18) { c.rs0[0] = c.rs0[1] = c.rs1[0] = c.rs1[1] = 0.f; }
  if constexpr (M >= 19) {
    constexpr int g = M - 19, e_lo = (32 * g) / 13, e_hi = (32 * (g + 1)) / 13;
    {
      constexpr int p_lo = g ? (32 * (g - 1)) / 13 : 0, p_hi = g ? (32 * g) / 13 : 0;
      if constexpr (p_lo + 0 < p_hi) { constexpr int e = p_lo + 0; if constexpr (e & 1) c.rs1[e >> 4] = fadd_s(c.rs1[e >> 4], c.S[X][0][e >> 4][e & 15]); else c.rs0[e >> 4] = fadd_s(c.rs0[e >> 4], c.S[X][0][e >> 4][e & 15]); }
      if constexpr (p_lo + 1 < p_hi) { constexpr int e = p_lo + 1; if constexpr (e & 1) c.rs1[e >> 4] = fadd_s(c.rs1[e >> 4], c.S[X][0][e >> 4][e & 15]); else c.rs0[e >> 4] = fadd_s(c.rs0[e >> 4], c.S[X][0][e >> 4][e & 15]); }
      if constexpr (p_lo + 2 < p_hi) { constexpr int e = p_lo + 2; if constexpr (e & 1) c.rs1[e >> 4] = fadd_s(c.rs1[e >> 4], c.S[X][0][e >> 4][e & 15]); else c.rs0[e >> 4] = fadd_s(c.rs0[e >> 4], c.S[X][0][e >> 4][e & 15]); }
    }
    if constexpr (e_lo + 0 < e_hi) c.S[X][0][(e_lo + 0) >> 4][(e_lo + 0) & 15] = __builtin_amdgcn_exp2f(c.S[X][0][(e_lo + 0) >> 4][(e_lo + 0) & 15]);
    if constexpr (e_lo + 1 < e_hi) c.S[X][0][(e_lo + 1) >> 4][(e_lo + 1) & 15] = __builtin_amdgcn_exp2f(c.S[X][0][(e_lo + 1) >> 4][(e_lo + 1) & 15]);
    if constexpr (e_lo + 2 < e_hi) c.S[X][0][(e_lo + 2) >> 4][(e_lo + 2) & 15] = __builtin_amdgcn_exp2f(c.S[X][0][(e_lo + 2) >> 4][(e_lo + 2) & 15]);
  }
  if constexpr (M == 31) asm volatile("" :: "v"(c.S[X][0][0]), "v"(c.S[X][0][1]), "v"(c.NEGM[0]), "v"(c.NEGM[1]), "v"(c.rs0[0]), "v"(c.rs0[1]), "v"(c.rs1[0]), "v"(c.rs1[1]));
  GAP();
}
template <int X, bool PEEL, bool ZERO1, int DK, int DV, bool KR, int... Ms> DI void phaseB_(W4C& c, const int t, iseq<Ms...>) {
  const unsigned kslot_rd = (unsigned)((t + 1) & 1) * 0x4000u;
  if constexpr (!PEEL) asm volatile("s_nop 1" ::: "memory");
  GAP();
  (gapB<X, PEEL, ZERO1, DK, DV, KR, Ms>(c, kslot_rd), ...);
  {
    #pragma unroll
    for (int e = (32 * 12) / 13; e < 32; ++e) { if (e & 1) c.rs1[e >> 4] = fadd_s(c.rs1[e >> 4], c.S[X][0][e >> 4][e & 15]); else c.rs0[e >> 4] = fadd_s(c.rs0[e >> 4], c.S[X][0][e >> 4][e & 15]); }
  }
  if constexpr (!PEEL) {
    if (__builtin_expect(c.pend, 0)) {
      float t0, t1;
      asm volatile("s_nop 7\n\ts_nop 7\n\t"
        "v_accvgpr_read_b32 %0, a0\n\tv_mul_f32 %0, %2, %0\n\tv_accvgpr_write_b32 a0, %0\n\t"
        "v_accvgpr_read_b32 %1, a1\n\tv_mul_f32 %1, %2, %1\n\tv_accvgpr_write_b32 a1, %1\n\t"
        "v_accvgpr_read_b32 %0, a2\n\tv_mul_f32 %0, %2, %0\n\tv_accvgpr_write_b32 a2, %0\n\t"
        "v_accvgpr_read_b32 %1, a3\n\tv_mul_f32 %1, %2, %1\n\tv_accvgpr_write_b32 a3, %1\n\t"
        "v_accvgpr_read_b32 %0, a4\n\tv_mul_f32 %0, %2, %0\n\tv_accvgpr_write_b32 a4, %0\n\t"
        "v_accvgpr_read_b32 %1, a5\n\tv_mul_f32 %1, %2, %1\n\tv_accvgpr_write_b32 a5, %1\n\t"
        "v_accvgpr_read_b32 %0, a6\n\tv_mul_f32 %0, %2, %0\n\tv_accvgpr_write_b32 a6, %0\n\t"
        "v_accvgpr_read_b32 %1, a7\n\tv_mul_f32 %1, %2, %1\n\tv_accvgpr_write_b32 a7, %1\n\t"
        "v_accvgpr_read_b32 %0, a8\n\tv_mul_f32 %0, %2, %0\n\tv_accvgpr_write_b32 a8, %0\n\t"
        "v_accvgpr_read_b32 %1, a9\n\tv_mul_f32 %1, %2, %1\n\tv_accvgpr_write_b32 a9, %1\n\t"
        "v_accvgpr_read_b32 %0, a10\n\tv_mul_f32 %0, %2, %0\n\tv_accvgpr_write_b32 a10, %0\n\t"
        "v_accvgpr_read_b32 %1, a11\n\tv_mul_f32 %1, %2, %1\n\tv_accvgpr_write_b32 a11, %1\n\t"
        "v_accvgpr_read_b32 %0, a12\n\tv_mul_f32 %0, %2, %0\n\tv_accvgpr_write_b32 a12, %0\n\t"
        "v_accvgpr_read_b32 %1, a13\n\tv_mul_f32 %1, %2, %1\n\tv_accvgpr_write_b32 a13, %1\n\t"
        "v_accvgpr_read_b32 %0, a14\n\tv_mul_f32 %0, %2, %0\n\tv_accvgpr_write_b32 a14, %0\n\t"
        "v_accvgpr_read_b32 %1, a15\n\tv_mul_f32 %1, %2, %1\n\tv_accvgpr_write_b32 a15, %1\n\t"
        "v_accvgpr_read_b32 %0, a16\n\tv_mul_f32 %0, %3, %0\n\tv_accvgpr_write_b32 a16, %0\n\t"
        "v_accvgpr_read_b32 %1, a17\n\tv_mul_f32 %1, %3, %1\n\tv_accvgpr_write_b32 a17, %1\n\t"
        "v_accvgpr_read_b32 %0, a18\n\tv_mul_f32 %0, %3, %0\n\tv_accvgpr_write_b32 a18, %0\n\t"
        "v_accvgpr_read_b32 %1, a19\n\tv_mul_f32 %1, %3, %1\n\tv_accvgpr_write_b32 a19, %1\n\t"
        "v_accvgpr_read_b32 %0, a20\n\tv_mul_f32 %0, %3, %0\n\tv_accvgpr_write_b32 a20, %0\n\t"
        "v_accvgpr_read_b32 %1, a21\n\tv_mul_f32 %1, %3, %1\n\tv_accvgpr_write_b32 a21, %1\n\t"
        "v_accvgpr_read_b32 %0, a22\n\tv_mul_f32 %0, %3, %0\n\tv_accvgpr_write_b32 a22, %0\n\t"
        "v_accvgpr_read_b32 %1, a23\n\tv_mul_f32 %1, %3, %1\n\tv_accvgpr_write_b32 a23, %1\n\t"
        "v_accvgpr_read_b32 %0, a24\n\tv_mul_f32 %0, %3, %0\n\tv_accvgpr_write_b32 a24, %0\n\t"
        "v_accvgpr_read_b32 %1, a25\n\tv_mul_f32 %1, %3, %1\n\tv_accvgpr_write_b32 a25, %1\n\t"
        "v_accvgpr_read_b32 %0, a26\n\tv_mul_f32 %0, %3, %0\n\tv_accvgpr_write_b32 a26, %0\n\t"
        "v_accvgpr_read_b32 %1, a27\n\tv_mul_f32 %1, %3, %1\n\tv_accvgpr_write_b32 a27, %1\n\t"
        "v_accvgpr_read_b32 %0, a28\n\tv_mul_f32 %0, %3, %0\n\tv_accvgpr_write_b32 a28, %0\n\t"
        "v_accvgpr_read_b32 %1, a29\n\tv_mul_f32 %1, %3, %1\n\tv_accvgpr_write_b32 a29, %1\n\t"
        "v_accvgpr_read_b32 %0, a30\n\tv_mul_f32 %0, %3, %0\n\tv_accvgpr_write_b32 a30, %0\n\t"
        "v_accvgpr_read_b32 %1, a31\n\tv_mul_f32 %1, %3, %1\n\tv_accvgpr_write_b32 a31, %1\n\t"
        "v_accvgpr_read_b32 %0, a32\n\tv_mul_f32 %0, %2, %0\n\tv_accvgpr_write_b32 a32, %0\n\t"
        "v_accvgpr_read_b32 %1, a33\n\tv_mul_f32 %1, %2, %1\n\tv_accvgpr_write_b32 a33, %1\n\t"
        "v_accvgpr_read_b32 %0, a34\n\tv_mul_f32 %0, %2, %0\n\tv_accvgpr_write_b32 a34, %0\n\t"
        "v_accvgpr_read_b32 %1, a35\n\tv_mul_f32 %1, %2, %1\n\tv_accvgpr_write_b32 a35, %1\n\t"
        "v_accvgpr_read_b32 %0, a36\n\tv_mul_f32 %0, %2, %0\n\tv_accvgpr_write_b32 a36, %0\n\t"
        "v_accvgpr_read_b32 %1, a37\n\tv_mul_f32 %1, %2, %1\n\tv_accvgpr_write_b32 a37, %1\n\t"
        "v_accvgpr_read_b32 %0, a38\n\tv_mul_f32 %0, %2, %0\n\tv_accvgpr_write_b32 a38, %0\n\t"
        "v_accvgpr_read_b32 %1, a39\n\tv_mul_f32 %1, %2, %1\n\tv_accvgpr_write_b32 a39, %1\n\t"
        "v_accvgpr_read_b32 %0, a40\n\tv_mul_f32 %0, %2, %0\n\tv_accvgpr_write_b32 a40, %0\n\t"
        "v_accvgpr_read_b32 %1, a41\n\tv_mul_f32 %1, %2, %1\n\tv_accvgpr_write_b32 a41, %1\n\t"
        "v_accvgpr_read_b32 %0, a42\n\tv_mul_f32 %0, %2, %0\n\tv_accvgpr_write_b32 a42, %0\n\t"
        "v_accvgpr_read_b32 %1, a43\n\tv_mul_f32 %1, %2, %1\n\tv_accvgpr_write_b32 a43, %1\n\t"
        "v_accvgpr_read_b32 %0, a44\n\tv_mul_f32 %0, %2, %0\n\tv_accvgpr_write_b32 a44, %0\n\t"
        "v_accvgpr_read_b32 %1, a45\n\tv_mul_f32 %1, %2, %1\n\tv_accvgpr_write_b32 a45, %1\n\t"
        "v_accvgpr_read_b32 %0, a46\n\tv_mul_f32 %0, %2, %0\n\tv_accvgpr_write_b32 a46, %0\n\t"
        "v_accvgpr_read_b32 %1, a47\n\tv_mul_f32 %1, %2, %1\n\tv_accvgpr_write_b32 a47, %1\n\t"
        "v_accvgpr_read_b32 %0, a48\n\tv_mul_f32 %0, %3, %0\n\tv_accvgpr_write_b32 a48, %0\n\t"
        "v_accvgpr_read_b32 %1, a49\n\tv_mul_f32 %1, %3, %1\n\tv_accvgpr_write_b32 a49, %1\n\t"
        "v_accvgpr_read_b32 %0, a50\n\tv_mul_f32 %0, %3, %0\n\tv_accvgpr_write_b32 a50, %0\n\t"
        "v_accvgpr_read_b32 %1, a51\n\tv_mul_f32 %1, %3, %1\n\tv_accvgpr_write_b32 a51, %1\n\t"
        "v_accvgpr_read_b32 %0, a52\n\tv_mul_f32 %0, %3, %0\n\tv_accvgpr_write_b32 a52, %0\n\t"
        "v_accvgpr_read_b32 %1, a53\n\tv_mul_f32 %1, %3, %1\n\tv_accvgpr_write_b32 a53, %1\n\t"
        "v_accvgpr_read_b32 %0, a54\n\tv_mul_f32 %0, %3, %0\n\tv_accvgpr_write_b32 a54, %0\n\t"
        "v_accvgpr_read_b32 %1, a55\n\tv_mul_f32 %1, %3, %1\n\tv_accvgpr_write_b32 a55, %1\n\t"
        "v_accvgpr_read_b32 %0, a56\n\tv_mul_f32 %0, %3, %0\n\tv_accvgpr_write_b32 a56, %0\n\t"
        "v_accvgpr_read_b32 %1, a57\n\tv_mul_f32 %1, %3, %1\n\tv_accvgpr_write_b32 a57, %1\n\t"
        "v_accvgpr_read_b32 %0, a58\n\tv_mul_f32 %0, %3, %0\n\tv_accvgpr_write_b32 a58, %0\n\t"
        "v_accvgpr_read_b32 %1, a59\n\tv_mul_f32 %1, %3, %1\n\tv_accvgpr_write_b32 a59, %1\n\t"
        "v_accvgpr_read_b32 %0, a60\n\tv_mul_f32 %0, %3, %0\n\tv_accvgpr_write_b32 a60, %0\n\t"
        "v_accvgpr_read_b32 %1, a61\n\tv_mul_f32 %1, %3, %1\n\tv_accvgpr_write_b32 a61, %1\n\t"
        "v_accvgpr_read_b32 %0, a62\n\tv_mul_f32 %0, %3, %0\n\tv_accvgpr_write_b32 a62, %0\n\t"
        "v_accvgpr_read_b32 %1, a63\n\tv_mul_f32 %1, %3, %1\n\tv_accvgpr_write_b32 a63, %1\n\t"
        "v_accvgpr_read_b32 %0, a64\n\tv_mul_f32 %0, %2, %0\n\tv_accvgpr_write_b32 a64, %0\n\t"
        "v_accvgpr_read_b32 %1, a65\n\tv_mul_f32 %1, %2, %1\n\tv_accvgpr_write_b32 a65, %1\n\t"
        "v_accvgpr_read_b32 %0, a66\n\tv_mul_f32 %0, %2, %0\n\tv_accvgpr_write_b32 a66, %0\n\t"
        "v_accvgpr_read_b32 %1, a67\n\tv_mul_f32 %1, %2, %1\n\tv_accvgpr_write_b32 a67, %1\n\t"
        "v_accvgpr_read_b32 %0, a68\n\tv_mul_f32 %0, %2, %0\n\tv_accvgpr_write_b32 a68, %0\n\t"
        "v_accvgpr_read_b32 %1, a69\n\tv_mul_f32 %1, %2, %1\n\tv_accvgpr_write_b32 a69, %1\n\t"
        "v_accvgpr_read_b32 %0, a70\n\tv_mul_f32 %0, %2, %0\n\tv_accvgpr_write_b32 a70, %0\n\t"
        "v_accvgpr_read_b32 %1, a71\n\tv_mul_f32 %1, %2, %1\n\tv_accvgpr_write_b32 a71, %1\n\t"
        "v_accvgpr_read_b32 %0, a72\n\tv_mul_f32 %0, %2, %0\n\tv_accvgpr_write_b32 a72, %0\n\t"
        "v_accvgpr_read_b32 %1, a73\n\tv_mul_f32 %1, %2, %1\n\tv_accvgpr_write_b32 a73, %1\n\t"
        "v_accvgpr_read_b32 %0, a74\n\tv_mul_f32 %0, %2, %0\n\tv_accvgpr_write_b32 a74, %0\n\t"
        "v_accvgpr_read_b32 %1, a75\n\tv_mul_f32 %1, %2, %1\n\tv_accvgpr_write_b32 a75, %1\n\t"
        "v_accvgpr_read_b32 %0, a76\n\tv_mul_f32 %0, %2, %0\n\tv_accvgpr_write_b32 a76, %0\n\t"
        "v_accvgpr_read_b32 %1, a77\n\tv_mul_f32 %1, %2, %1\n\tv_accvgpr_write_b32 a77, %1\n\t"
        "v_accvgpr_read_b32 %0, a78\n\tv_mul_f32 %0, %2, %0\n\tv_accvgpr_write_b32 a78, %0\n\t"
        "v_accvgpr_read_b32 %1, a79\n\tv_mul_f32 %1, %2, %1\n\tv_accvgpr_write_b32 a79, %1\n\t"
        "v_accvgpr_read_b32 %0, a80\n\tv_mul_f32 %0, %3, %0\n\tv_accvgpr_write_b32 a80, %0\n\t"
        "v_accvgpr_read_b32 %1, a81\n\tv_mul_f32 %1, %3, %1\n\tv_accvgpr_write_b32 a81, %1\n\t"
        "v_accvgpr_read_b32 %0, a82\n\tv_mul_f32 %0, %3, %0\n\tv_accvgpr_write_b32 a82, %0\n\t"
        "v_accvgpr_read_b32 %1, a83\n\tv_mul_f32 %1, %3, %1\n\tv_accvgpr_write_b32 a83, %1\n\t"
        "v_accvgpr_read_b32 %0, a84\n\tv_mul_f32 %0, %3, %0\n\tv_accvgpr_write_b32 a84, %0\n\t"
        "v_accvgpr_read_b32 %1, a85\n\tv_mul_f32 %1, %3, %1\n\tv_accvgpr_write_b32 a85, %1\n\t"
        "v_accvgpr_read_b32 %0, a86\n\tv_mul_f32 %0, %3, %0\n\tv_accvgpr_write_b32 a86, %0\n\t"
        "v_accvgpr_read_b32 %1, a87\n\tv_mul_f32 %1, %3, %1\n\tv_accvgpr_write_b32 a87, %1\n\t"
        "v_accvgpr_read_b32 %0, a88\n\tv_mul_f32 %0, %3, %0\n\tv_accvgpr_write_b32 a88, %0\n\t"
        "v_accvgpr_read_b32 %1, a89\n\tv_mul_f32 %1, %3, %1\n\tv_accvgpr_write_b32 a89, %1\n\t"
        "v_accvgpr_read_b32 %0, a90\n\tv_mul_f32 %0, %3, %0\n\tv_accvgpr_write_b32 a90, %0\n\t"
        "v_accvgpr_read_b32 %1, a91\n\tv_mul_f32 %1, %3, %1\n\tv_accvgpr_write_b32 a91, %1\n\t"
        "v_accvgpr_read_b32 %0, a92\n\tv_mul_f32 %0, %3, %0\n\tv_accvgpr_write_b32 a92, %0\n\t"
        "v_accvgpr_read_b32 %1, a93\n\tv_mul_f32 %1, %3, %1\n\tv_accvgpr_write_b32 a93, %1\n\t"
        "v_accvgpr_read_b32 %0, a94\n\tv_mul_f32 %0, %3, %0\n\tv_accvgpr_write_b32 a94, %0\n\t"
        "v_accvgpr_read_b32 %1, a95\n\tv_mul_f32 %1, %3, %1\n\tv_accvgpr_write_b32 a95, %1\n\t"
        "v_accvgpr_read_b32 %0, a96\n\tv_mul_f32 %0, %2, %0\n\tv_accvgpr_write_b32 a96, %0\n\t"
        "v_accvgpr_read_b32 %1, a97\n\tv_mul_f32 %1, %2, %1\n\tv_accvgpr_write_b32 a97, %1\n\t"
        "v_accvgpr_read_b32 %0, a98\n\tv_mul_f32 %0, %2, %0\n\tv_accvgpr_write_b32 a98, %0\n\t"
        "v_accvgpr_read_b32 %1, a99\n\tv_mul_f32 %1, %2, %1\n\tv_accvgpr_write_b32 a99, %1\n\t"
        "v_accvgpr_read_b32 %0, a100\n\tv_mul_f32 %0, %2, %0\n\tv_accvgpr_write_b32 a100, %0\n\t"
        "v_accvgpr_read_b32 %1, a101\n\tv_mul_f32 %1, %2, %1\n\tv_accvgpr_write_b32 a101, %1\n\t"
        "v_accvgpr_read_b32 %0, a102\n\tv_mul_f32 %0, %2, %0\n\tv_accvgpr_write_b32 a102, %0\n\t"
        "v_accvgpr_read_b32 %1, a103\n\tv_mul_f32 %1, %2, %1\n\tv_accvgpr_write_b32 a103, %1\n\t"
        "v_accvgpr_read_b32 %0, a104\n\tv_mul_f32 %0, %2, %0\n\tv_accvgpr_write_b32 a104, %0\n\t"
        "v_accvgpr_read_b32 %1, a105\n\tv_mul_f32 %1, %2, %1\n\tv_accvgpr_write_b32 a105, %1\n\t"
        "v_accvgpr_read_b32 %0, a106\n\tv_mul_f32 %0, %2, %0\n\tv_accvgpr_write_b32 a106, %0\n\t"
        "v_accvgpr_read_b32 %1, a107\n\tv_mul_f32 %1, %2, %1\n\tv_accvgpr_write_b32 a107, %1\n\t"
        "v_accvgpr_read_b32 %0, a108\n\tv_mul_f32 %0, %2, %0\n\tv_accvgpr_write_b32 a108, %0\n\t"
        "v_accvgpr_read_b32 %1, a109\n\tv_mul_f32 %1, %2, %1\n\tv_accvgpr_write_b32 a109, %1\n\t"
        "v_accvgpr_read_b32 %0, a110\n\tv_mul_f32 %0, %2, %0\n\tv_accvgpr_write_b32 a110, %0\n\t"
        "v_accvgpr_read_b32 %1, a111\n\tv_mul_f32 %1, %2, %1\n\tv_accvgpr_write_b32 a111, %1\n\t"
        "v_accvgpr_read_b32 %0, a112\n\tv_mul_f32 %0, %3, %0\n\tv_accvgpr_write_b32 a112, %0\n\t"
        "v_accvgpr_read_b32 %1, a113\n\tv_mul_f32 %1, %3, %1\n\tv_accvgpr_write_b32 a113, %1\n\t"
        "v_accvgpr_read_b32 %0, a114\n\tv_mul_f32 %0, %3, %0\n\tv_accvgpr_write_b32 a114, %0\n\t"
        "v_accvgpr_read_b32 %1, a115\n\tv_mul_f32 %1, %3, %1\n\tv_accvgpr_write_b32 a115, %1\n\t"
        "v_accvgpr_read_b32 %0, a116\n\tv_mul_f32 %0, %3, %0\n\tv_accvgpr_write_b32 a116, %0\n\t"
        "v_accvgpr_read_b32 %1, a117\n\tv_mul_f32 %1, %3, %1\n\tv_accvgpr_write_b32 a117, %1\n\t"
        "v_accvgpr_read_b32 %0, a118\n\tv_mul_f32 %0, %3, %0\n\tv_accvgpr_write_b32 a118, %0\n\t"
        "v_accvgpr_read_b32 %1, a119\n\tv_mul_f32 %1, %3, %1\n\tv_accvgpr_write_b32 a119, %1\n\t"
        "v_accvgpr_read_b32 %0, a120\n\tv_mul_f32 %0, %3, %0\n\tv_accvgpr_write_b32 a120, %0\n\t"
        "v_accvgpr_read_b32 %1, a121\n\tv_mul_f32 %1, %3, %1\n\tv_accvgpr_write_b32 a121, %1\n\t"
        "v_accvgpr_read_b32 %0, a122\n\tv_mul_f32 %0, %3, %0\n\tv_accvgpr_write_b32 a122, %0\n\t"
        "v_accvgpr_read_b32 %1, a123\n\tv_mul_f32 %1, %3, %1\n\tv_accvgpr_write_b32 a123, %1\n\t"
        "v_accvgpr_read_b32 %0, a124\n\tv_mul_f32 %0, %3, %0\n\tv_accvgpr_write_b32 a124, %0\n\t"
        "v_accvgpr_read_b32 %1, a125\n\tv_mul_f32 %1, %3, %1\n\tv_accvgpr_write_b32 a125, %1\n\t"
        "v_accvgpr_read_b32 %0, a126\n\tv_mul_f32 %0, %3, %0\n\tv_accvgpr_write_b32 a126, %0\n\t"
        "v_accvgpr_read_b32 %1, a127\n\tv_mul_f32 %1, %3, %1\n\tv_accvgpr_write_b32 a127, %1\n\t"
        "s_nop 1" : "=&v"(t0), "=&v"(t1) : "v"(c.fct[0]), "v"(c.fct[1]) : W4_ACLOB);
      c.lsum[0] = fmul_s(c.lsum[0], c.fct[0]); c.lsum[1] = fmul_s(c.lsum[1], c.fct[1]); c.pend = false;
    }
  }
  asm volatile("s_waitcnt lgkmcnt(0)" ::: "memory");
  GAP();
}
template <int X, bool PEEL, bool ZERO1, int DK = 1, int DV = 1, bool KR = true> DI void phaseB(W4C& c, const int t) { phaseB_<X, PEEL, ZERO1, DK, DV, KR>(c, t, make_iseq<32>{}); }

template <int... Is> DI void q_reads(W4C& c, unsigned qoff, iseq<Is...>) { (q_read<Is>(c, qoff), ...); }
template <int... Is> DI void k_reads(W4C& c, unsigned slot, iseq<Is...>) { (k_read<Is>(c, slot), ...); }
template <int... Is> DI void tr_reads(W4C& c, unsigned vslot, iseq<Is...>) { (tr_read<Is>(c, vslot), ...); }
template <int X, int... Cs> DI void packs(W4C& c, iseq<Cs...>) { (pack1<X, ((Cs >> 3) & 1), 2 * (Cs >> 4) + ((Cs >> 2) & 1), (Cs & 3)>(c), ...); }
template <int X, int J> DI void rowsum1_padded(W4C& c) { rowsum1<X, J>(c); asm volatile("s_nop 1"); }
template <int X, int... Js> DI void rowsums(W4C& c, iseq<Js...>) { (rowsum1_padded<X, Js>(c), ...); }
template <bool Z, int... Ms> DI void pv_all(W4C& c, iseq<Ms...>) { (pv_mfma<Ms, Z>(c), ...); }

typedef unsigned short u16;
#define GAS __attribute__((address_space(1)))
DI float bf2f(u16 v) { return __uint_as_float((unsigned)v << 16); }
DI float rbf(float x) { __bf16 b = (__bf16)x; return (float)b; }
DI void st16_wt(void* p, u32x4 v) {
  asm volatile("global_store_dwordx4 %0, %1, off sc1\n\ts_nop 1" :: "v"(p), "v"(v) : "memory");
}
DI float bflo(unsigned w) { return __uint_as_float(w << 16); }
DI float bfhi(unsigned w) { return __uint_as_float(w & 0xffff0000u); }

template <int CTRL> DI float dpp_add(float v) { return v + __int_as_float(__builtin_amdgcn_update_dpp(0, __float_as_int(v), CTRL, 0xf, 0xf, true)); }
DI float wave_sum(float v) {
  v = dpp_add<0xB1>(v); v = dpp_add<0x4E>(v); v = dpp_add<0x141>(v); v = dpp_add<0x140>(v);
  const float a = __int_as_float(__builtin_amdgcn_readlane(__float_as_int(v), 0)), b = __int_as_float(__builtin_amdgcn_readlane(__float_as_int(v), 16));
  const float c = __int_as_float(__builtin_amdgcn_readlane(__float_as_int(v), 32)), d = __int_as_float(__builtin_amdgcn_readlane(__float_as_int(v), 48));
  return (a + b) + (c + d);
}
DI void ln_mod_vals(const float (&xv)[8], const float (&shift)[8], const float (&scale)[8], unsigned (&out)[4]) {
  float s = 0.f;
  #pragma unroll
  for (int e = 0; e < 8; ++e) s += xv[e];
  const float mean = wave_sum(s) * (1.f / DM);
  float v = 0.f;
  #pragma unroll
  for (int e = 0; e < 8; ++e) { const float d = xv[e] - mean; v += d * d; }
  v = wave_sum(v);
  const float mb = rbf(mean), vb = rbf(v * (1.f / DM));
  const float sd = rbf(sqrtf(rbf(vb + 1e-5f)));
  float hv[8];
  #pragma unroll
  for (int e = 0; e < 8; ++e) {
    const float n = rbf(rbf(xv[e] - mb) / sd);
    hv[e] = rbf(n * rbf(1.f + scale[e])) + shift[e];
  }
  #pragma unroll
  for (int e = 0; e < 4; ++e) out[e] = cvtpk(hv[2 * e], hv[2 * e + 1]);
}
DI void ln_mod_row(const float (&xv)[8], const float* __restrict__ shift, const float* __restrict__ scale, int lane, unsigned (&out)[4]) {
  float sh[8], sc[8];
  #pragma unroll
  for (int e = 0; e < 8; ++e) { sh[e] = shift[lane * 8 + e]; sc[e] = scale[lane * 8 + e]; }
  ln_mod_vals(xv, sh, sc, out);
}

template <int M> DI void pv_drain1(W4C& c) { pv_mfma<M, false>(c); GAP(); }
template <int... Ms> DI void pv_drain(W4C& c, iseq<Ms...>) { (pv_drain1<Ms>(c), ...); }

#define AIMG 0x18000u
template <int NSTEP, int NEXTRA, int SOFF, class ISSUE, class HOOK> DI void proj64(f32x16 (&qa)[4][2], const unsigned ldsb, const unsigned lane, const unsigned uw, ISSUE issueW, HOOK hook, const bool abar) {
  const unsigned l32 = lane & 31u, hi = lane >> 5;
  unsigned ab[4];
  #pragma unroll
  for (int ks = 0; ks < 4; ++ks) ab[ks] = ldsb + AIMG + l32 * 128u + 16u * ((2u * ks + hi) ^ ((l32 >> 1) & 7u));
  const unsigned psw = ((lane & 1u) << 1) | ((lane >> 1) & 1u);
  unsigned wb[2];
  wb[0] = ldsb + 4096u * uw + hi * 2048u + ((lane >> 2) & 3u) * 64u + ((lane >> 4) & 1u) * 32u + psw * 8u;
  wb[1] = wb[0] + 0xC000u;
  bf16x8 af[2][2]; bf16x4 wl[2][4], wu[2][4];
  auto rdA = [&](int s, int slot, int b) { asm volatile("ds_read_b128 %0, %1 offset:%2" : "=v"(af[slot][b]) : "v"(ab[s & 3]), "i"((s >> 2) * 8192 + b * 4096) : "memory"); };
  auto rdW = [&](int s, int slot, int a) {
    const int rs = (s + SOFF) % 6;
    asm volatile("ds_read_b64_tr_b16 %0, %1 offset:%2" : "=v"(wl[slot][a]) : "v"(wb[rs / 3]), "i"((rs % 3) * 16384 + a * 512) : "memory");
    asm volatile("ds_read_b64_tr_b16 %0, %1 offset:%2" : "=v"(wu[slot][a]) : "v"(wb[rs / 3]), "i"((rs % 3) * 16384 + a * 512 + 256) : "memory");
  };
#define QW_WAIT(N, SL) do { asm volatile("s_waitcnt lgkmcnt(%0)" :: "i"(N) : "memory"); \
    asm volatile("" : "+v"(af[SL][0]), "+v"(af[SL][1])); \
    _Pragma("unroll") for (int a = 0; a < 4; ++a) asm volatile("" : "+v"(wl[SL][a]), "+v"(wu[SL][a])); } while (0)
  constexpr int LAST = NSTEP - 1 + NEXTRA;
  asm volatile("s_waitcnt vmcnt(20) lgkmcnt(0)" ::: "memory");
  if (abar) __builtin_amdgcn_s_barrier();
  rdA(0, 0, 0); rdA(0, 0, 1);
  #pragma unroll
  for (int a = 0; a < 4; ++a) rdW(0, 0, a);
  #pragma unroll
  for (int s = 0; s < NSTEP; ++s) {
    const int sl = s & 1;
    QW_WAIT(0, sl);
    if (s < NSTEP - 1) asm volatile("s_waitcnt vmcnt(%0)" :: "i"(4 * ((s + 5 < LAST ? s + 5 : LAST) - (s + 1))) : "memory");
    bf16x8 wf[4];
    #pragma unroll
    for (int a = 0; a < 4; ++a) wf[a] = __builtin_shufflevector(wl[sl][a], wu[sl][a], 0, 1, 2, 3, 4, 5, 6, 7);
    #pragma unroll
    for (int m = 0; m < 8; ++m) {
      const int a = m >> 1, b = m & 1;
      if (s == 0) asm volatile("s_nop 1\n\tv_mfma_f32_32x32x16_bf16 %0, %1, %2, 0" : "=&v"(qa[a][b]) : "v"(wf[a]), "v"(af[sl][b]));
      else        asm volatile("s_nop 1\n\tv_mfma_f32_32x32x16_bf16 %0, %1, %2, %0" : "+v"(qa[a][b]) : "v"(wf[a]), "v"(af[sl][b]));
      if (s < NSTEP - 1) { if (m == 0) { rdA(s + 1, sl ^ 1, 0); rdA(s + 1, sl ^ 1, 1); } else if (m < 5) rdW(s + 1, sl ^ 1, m - 1); }
      if (m >= 5 && m < 7) { if (s + 6 < NSTEP) { issueW(s + 6, 2 * (m - 5)); issueW(s + 6, 2 * (m - 5) + 1); } else if (m == 5 && s + 6 - NSTEP < NEXTRA) hook(s + 6 - NSTEP); }
    }
  }
#undef QW_WAIT
  asm volatile("s_nop 7\n\ts_nop 7\n\ts_nop 7" : "+v"(qa[0][0]), "+v"(qa[0][1]), "+v"(qa[1][0]), "+v"(qa[1][1]), "+v"(qa[2][0]), "+v"(qa[2][1]), "+v"(qa[3][0]), "+v"(qa[3][1]));
}

DI void store_part(const f32x16 (&pa)[4][2], char* lds, const unsigned uw, const unsigned lb, char* base  ) {
  const unsigned l32b = lb & 31u, hib = lb >> 5;
  __amdgpu_buffer_rsrc_t srdP = __builtin_amdgcn_make_buffer_rsrc((void*)(base + 256u * uw), (short)0, 63 * DM * 2 + 256, SRD_WORD3);
  char* stg = lds + 4096u * uw;
  #pragma unroll
  for (int b = 0; b < 2; ++b)
    #pragma unroll
    for (int a = 0; a < 4; ++a)
      #pragma unroll
      for (int q = 0; q < 2; ++q)
        *(u32x4*)(stg + 16384u * (2u * b + (l32b >> 4)) + 256u * (l32b & 15u) + 16u * ((4u * a + 2u * q + hib) ^ (l32b & 7u))) =
            u32x4{cvtpk(pa[a][b][8 * q], pa[a][b][8 * q + 1]), cvtpk(pa[a][b][8 * q + 2], pa[a][b][8 * q + 3]), cvtpk(pa[a][b][8 * q + 4], pa[a][b][8 * q + 5]), cvtpk(pa[a][b][8 * q + 6], pa[a][b][8 * q + 7])};
  asm volatile("s_waitcnt lgkmcnt(0)" ::: "memory");
  u32x4 v[16];
  #pragma unroll
  for (int j = 0; j < 16; ++j) { const unsigned r = 4u * j + (lb >> 4); v[j] = *(const u32x4*)(stg + 16384u * (r >> 4) + 256u * (r & 15u) + 16u * ((lb & 15u) ^ (r & 7u))); }
  #pragma unroll
  for (int j = 0; j < 16; ++j)
    asm volatile("buffer_store_dwordx4 %0, %1, %2, %3 offen sc1\n\ts_nop 1" :: "v"(v[j]), "v"((lb >> 4) * (DM * 2u) + 16u * (lb & 15u)), "s"(srdP), "s"(4u * j * (DM * 2u)) : "memory");
}

extern "C" __global__ __launch_bounds__(256, 1)
void attn_fwd(const unsigned short* __restrict__ Xb, const float* __restrict__ Mod, const unsigned short* __restrict__ Wq, const unsigned short* __restrict__ Kb, const unsigned short* __restrict__ Vb, float* __restrict__ Part,
              const unsigned short* __restrict__ Wo, const unsigned short* __restrict__ Wg, const unsigned short* __restrict__ Wu, const unsigned short* __restrict__ Wd,
              const unsigned short* __restrict__ Xin, unsigned short* __restrict__ X1) {
  extern __shared__ __attribute__((aligned(16))) char lds[];
  const unsigned tid = threadIdx.x, lane = tid & 63u, wid = tid >> 6, l32 = lane & 31u, hi = lane >> 5;
  constexpr int  T = NT, GQ = HQ / HKV;
  asm volatile("" : "+s"(Part), "+s"(Wo), "+s"(Wg), "+s"(Wu), "+s"(Wd), "+s"(Xin), "+s"(X1), "+s"(Mod));
  W4C c;
  c.T = T; c.THR = THR;
  c.ldsb = (unsigned)(size_t)lds;
  #pragma unroll
  for (int j = 0; j < 4; ++j) c.vkb[j] = c.ldsb + KRING + 256u * l32 + 16u * ((2u * j + hi) ^ (l32 & 7u));
  c.vvb = c.ldsb + VRING + (((lane & 3u) << 3) | (((lane >> 2) & 3u) << 6) | (((lane >> 4) & 1u) << 5) | (hi << 8));
  #pragma unroll
  for (int par = 0; par < 2; ++par) c.vksrc[par] = KRS * (lane >> 4) + 16u * ((lane & 15u) ^ (4u * par + (lane >> 4)));
  c.vvsrc = KRS * ((lane >> 2) & 7u) + 64u * hi + 16u * (lane & 3u);
  c.m0k = rfl(c.ldsb + KRING + 4096u * wid); c.m0v = rfl(c.ldsb + VRING + 4096u * wid);
  c.sofk = rfl(KRS * 16u * wid); c.sofv = rfl(KRS * 16u * wid);

  const unsigned ublk = blockIdx.x;
  const unsigned uw = rfl(wid);
  const unsigned kvh = ublk & 7u, h = kvh * GQ + uw;
  const unsigned row0 = 64u * (ublk >> 3);
  const char* Kbase = (const char*)Kb + (size_t)kvh * SEQ * 256;
  const char* Vbase = (const char*)Vb + (size_t)kvh * SEQ * 256;
  c.srdK = __builtin_amdgcn_make_buffer_rsrc((void*)Kbase, (short)0, SEQ * 256, SRD_WORD3);
  c.srdV = __builtin_amdgcn_make_buffer_rsrc((void*)Vbase, (short)0, SEQ * 256, SRD_WORD3);
  auto dma_k = [&](int tk) {
    const unsigned slot = (unsigned)(tk & 1) * 0x4000u, skv = KTILEB * (unsigned)tk;
    #pragma unroll
    for (int q = 0; q < 4; ++q)
      asm volatile("s_nop 4\n\ts_mov_b32 m0, %0\n\ts_nop 0\n\tbuffer_load_dwordx4 %1, %2, %3 offen lds"
                   :: "s"(rfl(c.m0k + slot + 1024u * q)), "v"(c.vksrc[q & 1]), "s"(c.srdK), "s"(rfl(skv + c.sofk + KRS * 4u * q)) : "m0", "memory");
  };
  auto dma_v = [&](int tv) {
    const unsigned slot = (unsigned)(tv & 1) * 0x4000u, skv = KTILEB * (unsigned)tv;
    #pragma unroll
    for (int q = 0; q < 4; ++q)
      asm volatile("s_nop 4\n\ts_mov_b32 m0, %0\n\ts_nop 0\n\tbuffer_load_dwordx4 %1, %2, %3 offen lds"
                   :: "s"(rfl(c.m0v + slot + 1024u * q)), "v"(c.vvsrc), "s"(c.srdV), "s"(rfl(skv + c.sofv + KRS * 8u * (q >> 1) + 128u * (q & 1))) : "m0", "memory");
  };
  const unsigned vwl = 64u * hi + 16u * (lane & 3u);
  {
    __amdgpu_buffer_rsrc_t srdW = __builtin_amdgcn_make_buffer_rsrc((void*)((const char*)Wq + (size_t)h * 256), (short)0, DM * HQ * 256 - h * 256, SRD_WORD3);
    const unsigned vw = ((lane >> 2) & 7u) * QRS + vwl;
    auto issueW = [&](int s, int i) {
      asm volatile("s_mov_b32 m0, %0\n\ts_nop 0\n\tbuffer_load_dwordx4 %1, %2, %3 offen lds"
                   :: "s"(c.ldsb + 4096u * uw + (unsigned)(((s + 4) % 6) * 16384 + i * 1024)), "v"(vw), "s"(srdW), "s"((unsigned)(16 * s + 8 * (i >> 1)) * QRS + (unsigned)(i & 1) * 128u) : "m0", "memory");
    };
    unsigned wtouch;
    { __amdgpu_buffer_rsrc_t srdT = __builtin_amdgcn_make_buffer_rsrc((void*)((const char*)Wq + (size_t)kvh * 1024u + (size_t)(16u * (ublk >> 3) + 4u * uw) * QRS), (short)0, 3 * QRS + 1024, SRD_WORD3);
      asm volatile("buffer_load_dword %0, %1, %2, 0 offen" : "=&v"(wtouch) : "v"((lane >> 4) * QRS + (lane & 15u) * 64u), "s"(srdT) : "memory"); }
    #pragma unroll
    for (int s = 0; s < 24; ++s) issueW(s >> 2, s & 3);
    {
      u32x4 xr[16];
      #pragma unroll
      for (int i = 0; i < 16; ++i) xr[i] = *(const u32x4*)(Xb + (size_t)(row0 + 16u * uw + i) * DM + lane * 8u);
      float sc1[8], sh[8];
      #pragma unroll
      for (int e = 0; e < 8; ++e) { sc1[e] = rbf(1.f + ((const GAS float*)Mod)[DM + lane * 8u + e]); sh[e] = ((const GAS float*)Mod)[lane * 8u + e]; }
      #pragma unroll
      for (int i = 0; i < 16; ++i) {
        u32x4 o;
        #pragma unroll
        for (int e = 0; e < 4; ++e) {
          const unsigned p = cvtpk(bflo(xr[i][e]) * sc1[2 * e], bfhi(xr[i][e]) * sc1[2 * e + 1]);
          o[e] = cvtpk(bflo(p) + sh[2 * e], bfhi(p) + sh[2 * e + 1]);
        }
        const unsigned r = 16u * uw + i;
        *(u32x4*)(lds + AIMG + 8192u * (lane >> 3) + 128u * r + 16u * ((lane & 7u) ^ ((r >> 1) & 7u))) = o;
      }
    }
    f32x16 qa[4][2];
    proj64<32, 3, 4>(qa, c.ldsb, lane, uw, issueW, [&](int e) { if (e == 0) dma_k(0); else if (e == 1) dma_k(1); else dma_v(0); }, true);
    asm volatile("" :: "v"(wtouch));
    #pragma unroll
    for (int qb = 0; qb < 2; ++qb)
      #pragma unroll
      for (int d0 = 0; d0 < 8; ++d0) {
        u32x4 w;
        #pragma unroll
        for (int e = 0; e < 4; ++e) w[e] = cvtpk(fmul_s(qa[d0 >> 1][qb][8 * (d0 & 1) + 2 * e], 0.12751743082f), fmul_s(qa[d0 >> 1][qb][8 * (d0 & 1) + 2 * e + 1], 0.12751743082f));
        q_write(qb, d0, w);
      }
  }
  asm volatile("s_waitcnt vmcnt(0)\n\ts_barrier" ::: "memory");
  k_reads(c, 0u, make_iseq<16>{});
  asm volatile("s_waitcnt lgkmcnt(0)\n\ts_nop 1" ::: "memory");

  c.mhat[0] = c.mhat[1] = 0.f; c.lsum[0] = c.lsum[1] = 0.f; c.fct[0] = c.fct[1] = 1.f; c.pend = false;

  phaseA<0, true>(c, 0);
  {
    __amdgpu_buffer_rsrc_t srdT = __builtin_amdgcn_make_buffer_rsrc((void*)((const char*)Wo + (size_t)kvh * (512u * 1024u) + (size_t)(ublk >> 3) * 16384u), (short)0, 16384, SRD_WORD3);
    asm volatile("s_mov_b32 m0, %0\n\ts_nop 0\n\tbuffer_load_dword %1, %2, 0 offen lds" :: "s"(c.ldsb + AIMG + 16384u * uw), "v"(tid * 64u), "s"(srdT) : "m0", "memory");
  }
  phaseB<0, true,  false>(c, 0);
  phaseA<1, false>(c, 1); phaseB<1, false, true>(c, 1);
  for (int t = 2; t < T - 2; t += 2) {
    phaseA<0, false>(c, t);     phaseB<0, false, false>(c, t);
    phaseA<1, false>(c, t + 1); phaseB<1, false, false>(c, t + 1);
  }
  phaseA<0, false>(c, T - 2); phaseB<0, false, false,  0,  1,  true>(c, T - 2);
  __amdgpu_buffer_rsrc_t srdWo = __builtin_amdgcn_make_buffer_rsrc((void*)((const char*)Wo + (size_t)kvh * (512u * 1024u) + 256u * uw), (short)0, 512 * 1024 - 256 * (int)uw, SRD_WORD3);
  const unsigned vwo = ((lane >> 2) & 7u) * 1024u + vwl;
  auto issueWo = [&](int s, int i) {
    asm volatile("s_mov_b32 m0, %0\n\ts_nop 0\n\tbuffer_load_dwordx4 %1, %2, %3 offen lds"
                 :: "s"(c.ldsb + 4096u * uw + (unsigned)(((s + 4) % 6) * 16384 + i * 1024)), "v"(vwo), "s"(srdWo), "s"((unsigned)(16 * s + 8 * (i >> 1)) * 1024u + (unsigned)(i & 1) * 128u) : "m0", "memory");
  };
  auto issueWo4 = [&](int s) { issueWo(s, 0); issueWo(s, 1); issueWo(s, 2); issueWo(s, 3); };
  {
    const unsigned short* Wm = uw == 0u ? Wg : uw == 1u ? Wu : Wd;
    __amdgpu_buffer_rsrc_t srdM = __builtin_amdgcn_make_buffer_rsrc((void*)((const char*)Wm + (size_t)ublk * 7168u), (short)0, uw < 3u ? 7168 : 0, SRD_WORD3);
    const unsigned pm = c.ldsb + AIMG + 16384u * uw;
    asm volatile("s_mov_b32 m0, %0\n\ts_nop 0\n\tbuffer_load_dword %1, %2, 0 offen lds" :: "s"(pm), "v"(lane * 64u), "s"(srdM) : "m0", "memory");
    asm volatile("s_mov_b32 m0, %0\n\ts_nop 0\n\tbuffer_load_dword %1, %2, 0 offen lds" :: "s"(pm + 256u), "v"(lane * 64u + 4096u), "s"(srdM) : "m0", "memory");
  }
  issueWo4(0); issueWo4(1);
  phaseA<1, false, false, false,  10>(c, T - 1);
  issueWo4(2); issueWo4(3); issueWo4(4);
  phaseB<1, false, false,  0,  0,  false>(c, T - 1);
  {
    const unsigned vslot = (unsigned)((T - 1) & 1) * 0x4000u;
    tr_reads(c, vslot, make_iseq<32>{});
    #pragma unroll
    for (int e = 0; e < 32; ++e) c.S[1][1][e >> 4][e & 15] = __builtin_amdgcn_exp2f(c.S[1][1][e >> 4][e & 15]);
    GAP(); asm volatile("s_nop 1" ::: "memory"); GAP();
    packs<1>(c, make_iseq<32>{});
    rowsums<1>(c, iseq<8, 9, 10, 11, 12, 13, 14, 15, 24, 25, 26, 27, 28, 29, 30, 31>{});
    #pragma unroll
    for (int qb = 0; qb < 2; ++qb) { const float rs = fadd_s(c.rs0[qb], c.rs1[qb]);
      auto pr = __builtin_amdgcn_permlane32_swap(__float_as_uint(rs), __float_as_uint(rs), false, false);
      c.lsum[qb] = fadd_s(c.lsum[qb], fadd_s(__uint_as_float(pr[0]), __uint_as_float(pr[1]))); }
    asm volatile("s_waitcnt lgkmcnt(0)" : W4_VLO :: "memory"); asm volatile("" : W4_VUP :: "memory");
    GAP();
    #pragma unroll
    for (int ks = 0; ks < 4; ++ks)
      #pragma unroll
      for (int db = 0; db < 4; ++db) c.Vf[db][ks] = __builtin_shufflevector(c.vlo[db][ks], c.vup[db][ks], 0, 1, 2, 3, 4, 5, 6, 7);
    asm volatile("s_nop 1" ::: "memory");
    pv_drain(c, make_iseq<32>{});
    asm volatile("s_nop 7\n\ts_nop 7" ::: "memory");
  }
  {
    unsigned lb;
    asm volatile("v_mbcnt_lo_u32_b32 %0, -1, 0\n\tv_mbcnt_hi_u32_b32 %0, -1, %0" : "=v"(lb));
    const unsigned l32b = lb & 31u, hib = lb >> 5;
    char* oim = lds + AIMG + 16384u * uw;
    asm volatile("s_waitcnt vmcnt(20)" ::: "memory");
    #pragma unroll
    for (int qb = 0; qb < 2; ++qb) {
      const float il = c.lsum[qb] > 0.f ? __builtin_amdgcn_rcpf(c.lsum[qb]) : 0.f;
      #pragma unroll
      for (int db = 0; db < 4; ++db) {
        float o[16];
        W4_READ_O(db, qb, o);
        #pragma unroll
        for (int gq = 0; gq < 4; ++gq) {
          u32x2 w = {cvtpk(fmul_s(o[4 * gq + 0], il), fmul_s(o[4 * gq + 1], il)), cvtpk(fmul_s(o[4 * gq + 2], il), fmul_s(o[4 * gq + 3], il))};
          *(u32x2*)(oim + 8192u * (db >> 1) + 128u * (32u * qb + l32b) + 16u * ((4u * (db & 1) + gq) ^ ((l32b >> 1) & 7u)) + 8u * hib) = w;
        }
      }
    }
    asm volatile("s_waitcnt lgkmcnt(0)\n\ts_barrier" ::: "memory");
    issueWo4(5);
    f32x16 pa[4][2];
    proj64<32, 0, 4>(pa, c.ldsb, lb, uw, issueWo, [&](int) {}, false);
    const unsigned rr = row0 + 8u * kvh + 2u * uw;
    u32x4 xw[2];
    f32x4 mv[3][2];
    #pragma unroll
    for (int i = 0; i < 2; ++i) xw[i] = *(const GAS u32x4*)((const GAS unsigned short*)Xin + (size_t)(rr + i) * DM + lb * 8u);
    #pragma unroll
    for (int j = 0; j < 3; ++j)
      #pragma unroll
      for (int i = 0; i < 2; ++i) mv[j][i] = *(const GAS f32x4*)((const GAS float*)Mod + (2 + j) * DM + lb * 8u + 4 * i);
    store_part(pa, lds, uw, lb, (char*)Part + ((size_t)kvh * SEQ + row0) * (DM * 2u));
    asm volatile("s_waitcnt vmcnt(0)" : "+v"(xw[0]), "+v"(xw[1]), "+v"(mv[0][0]), "+v"(mv[0][1]), "+v"(mv[1][0]), "+v"(mv[1][1]), "+v"(mv[2][0]), "+v"(mv[2][1]) :: "memory");
    if (lb == 0) {
      GAS unsigned* cnt = (GAS unsigned*)(Mod + 6 * DM) + 32u * (ublk >> 3);
      unsigned seen = __hip_atomic_fetch_add(cnt, 1u, __ATOMIC_RELAXED, __HIP_MEMORY_SCOPE_AGENT) + 1u;
      for (unsigned n = 0; n < (1u << 20) && seen < 4u * HKV; ++n) { __builtin_amdgcn_s_sleep(1); seen = __hip_atomic_load(cnt, __ATOMIC_RELAXED, __HIP_MEMORY_SCOPE_AGENT); }
    }
    asm volatile("s_waitcnt vmcnt(0)" ::: "memory");
    u32x4 pp[2][8];
    __amdgpu_buffer_rsrc_t srdR = __builtin_amdgcn_make_buffer_rsrc((void*)((char*)Part + (size_t)rr * (DM * 2u)), (short)0, (HKV - 1) * SEQ * DM * 2 + 2 * DM * 2, SRD_WORD3);
    #pragma unroll
    for (int i = 0; i < 2; ++i)
      #pragma unroll
      for (int g = 0; g < 8; ++g)
        asm volatile("buffer_load_dwordx4 %0, %1, %2, %3 offen sc1" : "=&v"(pp[i][g]) : "v"(lb * 16u), "s"(srdR), "s"((unsigned)g * (SEQ * DM * 2u) + (unsigned)i * (DM * 2u)) : "memory");
    asm volatile("s_waitcnt vmcnt(0)" : "+v"(pp[0][0]), "+v"(pp[0][1]), "+v"(pp[0][2]), "+v"(pp[0][3]), "+v"(pp[0][4]), "+v"(pp[0][5]), "+v"(pp[0][6]), "+v"(pp[0][7]),
                                        "+v"(pp[1][0]), "+v"(pp[1][1]), "+v"(pp[1][2]), "+v"(pp[1][3]), "+v"(pp[1][4]), "+v"(pp[1][5]), "+v"(pp[1][6]), "+v"(pp[1][7]) :: "memory");
    #pragma unroll
    for (int i = 0; i < 2; ++i) {
      float a[8] = {0, 0, 0, 0, 0, 0, 0, 0};
      #pragma unroll
      for (int g = 0; g < 8; ++g)
        #pragma unroll
        for (int e = 0; e < 4; ++e) { a[2 * e] += bflo(pp[i][g][e]); a[2 * e + 1] += bfhi(pp[i][g][e]); }
      float xv[8];
      #pragma unroll
      for (int e = 0; e < 8; ++e) {
        const float xe = (e & 1) ? bfhi(xw[i][e >> 1]) : bflo(xw[i][e >> 1]);
        xv[e] = rbf(xe + rbf(mv[0][e >> 2][e & 3] * rbf(a[e])));
      }
      st16_wt(X1 + (size_t)(rr + i) * DM + lb * 8u, u32x4{cvtpk(xv[0], xv[1]), cvtpk(xv[2], xv[3]), cvtpk(xv[4], xv[5]), cvtpk(xv[6], xv[7])});
      unsigned o[4];
      float sh[8], sc[8];
      #pragma unroll
      for (int e = 0; e < 8; ++e) { sh[e] = mv[1][e >> 2][e & 3]; sc[e] = mv[2][e >> 2][e & 3]; }
      ln_mod_vals(xv, sh, sc, o);
      st16_wt(X1 + (size_t)SEQ * DM + (size_t)(rr + i) * DM + lb * 8u, u32x4{o[0], o[1], o[2], o[3]});
    }
  }
}

__global__ __launch_bounds__(256) void pre_kernel(const u16* __restrict__ c, const u16* __restrict__ wmod, float* __restrict__ mod, const u16* __restrict__ x, u16* __restrict__ xn) {
  __shared__ float red[64][33];
  const int tid = threadIdx.x;
  if (blockIdx.x >= 96) {
    const int lane = tid & 63, row = (blockIdx.x - 96) * 4 + (tid >> 6);
    const u32x4 w = *(const u32x4*)(x + (size_t)row * DM + lane * 8);
    float xv[8];
    #pragma unroll
    for (int e = 0; e < 4; ++e) { xv[2 * e] = bflo(w[e]); xv[2 * e + 1] = bfhi(w[e]); }
    float s = 0.f;
    #pragma unroll
    for (int e = 0; e < 8; ++e) s += xv[e];
    const float mean = wave_sum(s) * (1.f / DM);
    float v = 0.f;
    #pragma unroll
    for (int e = 0; e < 8; ++e) { const float d = xv[e] - mean; v += d * d; }
    v = wave_sum(v);
    const float mb = rbf(mean), vb = rbf(v * (1.f / DM));
    const float sd = rbf(sqrtf(rbf(vb + 1e-5f)));
    u32x4 o;
    #pragma unroll
    for (int e = 0; e < 4; ++e) o[e] = cvtpk(rbf(xv[2 * e] - mb) / sd, rbf(xv[2 * e + 1] - mb) / sd);
    st16_wt(xn + (size_t)row * DM + lane * 8, o);
    return;
  }
  if (blockIdx.x == 0 && tid < 32) ((unsigned*)(mod + 6 * DM))[32 * tid] = 0u;
  const int kg = tid >> 2, cq = tid & 3, col0 = blockIdx.x * 32 + cq * 8;
  float acc[8] = {0, 0, 0, 0, 0, 0, 0, 0};
  #pragma unroll
  for (int i = 0; i < 8; ++i) {
    const int k = kg * 8 + i;
    const float cv = bf2f(c[k]);
    const float s = rbf(cv / (1.f + __expf(-cv)));
    const u32x4 w = *(const u32x4*)(wmod + (size_t)k * (6 * DM) + col0);
    #pragma unroll
    for (int e = 0; e < 4; ++e) { acc[2 * e] += s * bflo(w[e]); acc[2 * e + 1] += s * bfhi(w[e]); }
  }
  #pragma unroll
  for (int e = 0; e < 8; ++e) red[kg][cq * 8 + e] = acc[e];
  __syncthreads();
  if (tid < 32) {
    float s = 0.f;
    for (int g = 0; g < 64; ++g) s += red[g][tid];
    mod[blockIdx.x * 32 + tid] = rbf(s);
  }
}

enum { EPI_Q = 0, EPI_PART = 1, EPI_GEGLU = 2, EPI_DOWN = 3 };
struct GemmArgs { const u16* A; const u16* B; const u16* B2; void* C; const u16* res; const float* mod; };

DI float gelu_tanh(float g) {
  const float y2 = g * (2.302208198f + 0.1029432397f * g * g);
  return g * __builtin_amdgcn_rcpf(1.f + __builtin_amdgcn_exp2f(-y2));
}

typedef float f32x2v __attribute__((ext_vector_type(2)));
DI f32x2v rbf2(f32x2v v) { const unsigned w = cvtpk(v[0], v[1]); return f32x2v{bflo(w), bfhi(w)}; }
DI unsigned geglu2(f32x2v g_, f32x2v u_) {
  const f32x2v g = rbf2(g_), u = rbf2(u_);
  const f32x2v y2 = g * (g * g * 0.1029432397f + 2.302208198f);
  const f32x2v d = f32x2v{__builtin_amdgcn_exp2f(-y2[0]), __builtin_amdgcn_exp2f(-y2[1])} + 1.f;
  const f32x2v o = rbf2(g * f32x2v{__builtin_amdgcn_rcpf(d[0]), __builtin_amdgcn_rcpf(d[1])}) * u;
  return cvtpk(o[0], o[1]);
}

template <int BM, int BN, int WM, int WN, int NST, int EPI, int LDA, int LDB, int LDC, int KT, int NTM, int NTN>
__global__ __launch_bounds__(64 * WM * WN, 1) void gemm_kernel(const u16* __restrict__ A, const u16* __restrict__ B, const u16* __restrict__ B2, void* __restrict__ C,
                                                                const u16* __restrict__ res, const float* __restrict__ mod) {
  extern __shared__ __attribute__((aligned(16))) char lds[];
  constexpr int NW = WM * WN, TM = BM / WM / 32, TN = BN / WN / 32;
  constexpr bool DUAL = (EPI == EPI_GEGLU);
  constexpr int PA = BM / 8, PB = BN / 8, PAW = PA / NW, PBW = PB / NW, PPW = PAW + PBW;
  constexpr int ABYTES = BM * 128, BBYTES = BN * 128, SBYTES = ABYTES + BBYTES;
  static_assert(PA % NW == 0 && PB % NW == 0, "pieces divide over waves");
  static_assert(!DUAL || TN == 2, "dual: tn 0 = gate, tn 1 = up");
  asm volatile("" : "+s"(C), "+s"(res), "+s"(mod));
  const unsigned tid = threadIdx.x, lane = tid & 63u, l32 = lane & 31u, hi = lane >> 5;
  const unsigned wid = rfl(tid >> 6), wm = wid % WM, wn = wid / WM;
  const unsigned ldsb = (unsigned)(size_t)lds;
  unsigned bid = blockIdx.x;
  { constexpr unsigned nwg = 0; (void)nwg; }
  const unsigned G = gridDim.x;
  bid = (G % 8u == 0u) ? (bid % 8u) * (G / 8u) + bid / 8u : bid;
  const unsigned tn_ = (EPI == EPI_Q) ? bid / NTM : bid % NTN, rest = bid / NTN, tm_ = (EPI == EPI_Q) ? bid % NTM : rest % NTM, sp = (EPI == EPI_Q) ? 0u : rest / NTM;
  const unsigned m0 = tm_ * BM, k0 = sp * KT * 64u;
  const unsigned n0 = tn_ * (DUAL ? BN / 2 : BN);
  __amdgpu_buffer_rsrc_t srdA = __builtin_amdgcn_make_buffer_rsrc((void*)((const char*)A + ((size_t)m0 * LDA + k0) * 2), (short)0, 0x7fffffff, SRD_WORD3);
  __amdgpu_buffer_rsrc_t srdB = __builtin_amdgcn_make_buffer_rsrc((void*)((const char*)B + ((size_t)k0 * LDB + n0) * 2), (short)0, 0x7fffffff, SRD_WORD3);
  __amdgpu_buffer_rsrc_t srdB2 = __builtin_amdgcn_make_buffer_rsrc((void*)((const char*)(DUAL ? B2 : B) + ((size_t)k0 * LDB + n0) * 2), (short)0, 0x7fffffff, SRD_WORD3);
  unsigned va[2];
  #pragma unroll
  for (int par = 0; par < 2; ++par) va[par] = (lane >> 3) * (LDA * 2u) + 16u * ((lane & 7u) ^ ((4u * par + (lane >> 4)) & 7u));
  const unsigned vb = ((lane >> 2) & 7u) * (LDB * 2u) + 64u * hi + 16u * (lane & 3u);
  auto issue = [&](int t, int buf) {
    const unsigned sb = ldsb + (unsigned)buf * SBYTES;
    #pragma unroll
    for (int i = 0; i < PAW; ++i) {
      const unsigned p = wid * PAW + i;
      asm volatile("s_mov_b32 m0, %0\n\ts_nop 0\n\tbuffer_load_dwordx4 %1, %2, %3 offen lds"
                   :: "s"(sb + 1024u * p), "v"((PAW % 2 == 0) ? va[i & 1] : ((p & 1u) ? va[1] : va[0])), "s"(srdA), "s"(p * 8u * (LDA * 2u) + (unsigned)t * 128u) : "m0", "memory");
    }
    #pragma unroll
    for (int i = 0; i < PBW; ++i) {
      const unsigned p = wid * PBW + i, rg = p / (BN / 64), cb2 = p % (BN / 64);
      if constexpr (DUAL) {
        constexpr unsigned HB = BN / 128;
        const bool second = cb2 >= HB; const unsigned cc = second ? cb2 - HB : cb2;
        const unsigned so = ((unsigned)t * 64u + rg * 8u) * (LDB * 2u) + cc * 128u, md = sb + ABYTES + (rg * (BN / 32) + 2u * cb2) * 512u;
        if (second) asm volatile("s_mov_b32 m0, %0\n\ts_nop 0\n\tbuffer_load_dwordx4 %1, %2, %3 offen lds" :: "s"(md), "v"(vb), "s"(srdB2), "s"(so) : "m0", "memory");
        else        asm volatile("s_mov_b32 m0, %0\n\ts_nop 0\n\tbuffer_load_dwordx4 %1, %2, %3 offen lds" :: "s"(md), "v"(vb), "s"(srdB), "s"(so) : "m0", "memory");
      } else {
        asm volatile("s_mov_b32 m0, %0\n\ts_nop 0\n\tbuffer_load_dwordx4 %1, %2, %3 offen lds"
                     :: "s"(sb + ABYTES + (rg * (BN / 32) + 2u * cb2) * 512u), "v"(vb), "s"(srdB), "s"(((unsigned)t * 64u + rg * 8u) * (LDB * 2u) + cb2 * 128u) : "m0", "memory");
      }
    }
  };
  unsigned ab[4];
  #pragma unroll
  for (int ks = 0; ks < 4; ++ks) ab[ks] = ldsb + (wm * (TM * 32u) + l32) * 128u + 16u * ((2u * ks + hi) ^ ((l32 >> 1) & 7u));
  const unsigned cbase = DUAL ? wn : wn * TN;
  constexpr unsigned CSTEP = DUAL ? (BN / 64) : 1;
  const unsigned bb = ldsb + ABYTES + (hi * (BN / 32) + cbase) * 512u + ((lane >> 2) & 3u) * 64u + ((lane >> 4) & 1u) * 32u + (lane & 3u) * 8u;

  f32x16 acc[TN][TM];
  #pragma unroll
  for (int a = 0; a < TN; ++a)
    #pragma unroll
    for (int b = 0; b < TM; ++b) acc[a][b] = f32x16{};

  constexpr int NOUT = (BM * ((DUAL ? BN / 2 : BN) / 8)) / (64 * NW);
  u32x4 rres[NOUT]; f32x4 rgb[2];
  if constexpr (EPI == EPI_DOWN) {
    static_assert((64 * NW) % (BN / 8) == 0, "same column chunk for every output of a thread");
    #pragma unroll
    for (int i = 0; i < NOUT; ++i) rres[i] = *(const GAS u32x4*)((const GAS u16*)res + (size_t)(m0 + (i * (64u * NW) + tid) / (BN / 8)) * LDC + n0 + (tid % (BN / 8)) * 8u);
    #pragma unroll
    for (int i = 0; i < 2; ++i) rgb[i] = *(const GAS f32x4*)((const GAS float*)mod + 5 * DM + n0 + (tid % (BN / 8)) * 8u + 4 * i);
  }
  #pragma unroll
  for (int s = 0; s < NST - 1; ++s) issue(s, s);
  bf16x8 af[2][TM]; bf16x4 wl[2][TN], wu[2][TN];
  auto rd = [&](int ks, int slot, unsigned soff) {
    #pragma unroll
    for (int b = 0; b < TM; ++b) asm volatile("ds_read_b128 %0, %1 offset:%2" : "=v"(af[slot][b]) : "v"(ab[ks] + soff), "i"(b * 4096) : "memory");
    #pragma unroll
    for (int a = 0; a < TN; ++a) {
      asm volatile("ds_read_b64_tr_b16 %0, %1 offset:%2" : "=v"(wl[slot][a]) : "v"(bb + soff), "i"((2 * ks * (BN / 32) + a * CSTEP) * 512) : "memory");
      asm volatile("ds_read_b64_tr_b16 %0, %1 offset:%2" : "=v"(wu[slot][a]) : "v"(bb + soff), "i"((2 * ks * (BN / 32) + a * CSTEP) * 512 + 256) : "memory");
    }
  };
  auto mm = [&](int slot) {
    #pragma unroll
    for (int a = 0; a < TN; ++a) {
      const bf16x8 wf = __builtin_shufflevector(wl[slot][a], wu[slot][a], 0, 1, 2, 3, 4, 5, 6, 7);
      #pragma unroll
      for (int b = 0; b < TM; ++b) acc[a][b] = __builtin_amdgcn_mfma_f32_32x32x16_bf16(wf, af[slot][b], acc[a][b], 0, 0, 0);
    }
  };
  constexpr int RPK = TM + 2 * TN;
#define GW_WAIT(N, SL) do { asm volatile("s_waitcnt lgkmcnt(%0)" :: "i"(N) : "memory"); \
    _Pragma("unroll") for (int b = 0; b < TM; ++b) asm volatile("" : "+v"(af[SL][b])); \
    _Pragma("unroll") for (int a = 0; a < TN; ++a) asm volatile("" : "+v"(wl[SL][a]), "+v"(wu[SL][a])); } while (0)
  asm volatile("s_waitcnt vmcnt(%0)" :: "i"(PPW * (NST - 2)) : "memory");
  __builtin_amdgcn_s_barrier();
  rd(0, 0, 0u); rd(1, 1, 0u);
  int buf = 0;
  for (int t = 0; t < KT; ++t) {
    const unsigned soff = (unsigned)buf * SBYTES;
    const int nbuf = (buf + 1 == NST) ? 0 : buf + 1;
    const unsigned noff = (unsigned)nbuf * SBYTES;
    GW_WAIT(RPK, 0); mm(0); rd(2, 0, soff);
    GW_WAIT(RPK, 1); mm(1); rd(3, 1, soff);
    if (t + 1 < KT) {
      if (t + NST - 2 < KT) asm volatile("s_waitcnt vmcnt(%0)" :: "i"(PPW * (NST - 3)) : "memory");
      else {
        #pragma unroll
        for (int r = 0; r < NST - 3; ++r) if (KT - t - 2 == r) asm volatile("s_waitcnt vmcnt(%0)" :: "i"(PPW * r) : "memory");
      }
      __builtin_amdgcn_s_barrier();
      if (t + NST - 1 < KT) issue(t + NST - 1, (buf + NST - 1) % NST);
      GW_WAIT(RPK, 0); mm(0); rd(0, 0, noff);
      GW_WAIT(RPK, 1); mm(1); rd(1, 1, noff);
    } else {
      GW_WAIT(RPK, 0); mm(0);
      GW_WAIT(0, 1); mm(1);
    }
    buf = nbuf;
  }
#undef GW_WAIT
  if constexpr (EPI == EPI_PART) {
    float* Cp = (float*)C + (size_t)sp * SEQ * LDC;
    #pragma unroll
    for (int b = 0; b < TM; ++b) {
      const unsigned row = m0 + wm * (TM * 32u) + b * 32u + l32;
      #pragma unroll
      for (int a = 0; a < TN; ++a)
        #pragma unroll
        for (int g = 0; g < 4; ++g)
          *(f32x4*)(Cp + (size_t)row * LDC + n0 + (cbase + a) * 32u + 8u * g + 4u * hi) = f32x4{acc[a][b][4 * g], acc[a][b][4 * g + 1], acc[a][b][4 * g + 2], acc[a][b][4 * g + 3]};
    }
  } else {
    constexpr int OUTW = DUAL ? BN / 2 : BN, RSTR = OUTW * 2 + 16;
    __builtin_amdgcn_s_barrier();
    #pragma unroll
    for (int b = 0; b < TM; ++b) {
      const unsigned rl = wm * (TM * 32u) + b * 32u + l32;
      if constexpr (DUAL) {
        #pragma unroll
        for (int g = 0; g < 4; ++g) {
          *(u32x2*)(lds + rl * RSTR + (wn * 32u + 8u * g + 4u * hi) * 2u) =
              u32x2{geglu2(f32x2v{acc[0][b][4 * g], acc[0][b][4 * g + 1]}, f32x2v{acc[1][b][4 * g], acc[1][b][4 * g + 1]}),
                    geglu2(f32x2v{acc[0][b][4 * g + 2], acc[0][b][4 * g + 3]}, f32x2v{acc[1][b][4 * g + 2], acc[1][b][4 * g + 3]})};
        }
      } else {
        #pragma unroll
        for (int a = 0; a < TN; ++a)
          #pragma unroll
          for (int g = 0; g < 4; ++g) {
            float hv[4];
            #pragma unroll
            for (int e = 0; e < 4; ++e) hv[e] = (EPI == EPI_Q) ? acc[a][b][4 * g + e] * 0.12751743082f : acc[a][b][4 * g + e];
            *(u32x2*)(lds + rl * RSTR + ((cbase + a) * 32u + 8u * g + 4u * hi) * 2u) = u32x2{cvtpk(hv[0], hv[1]), cvtpk(hv[2], hv[3])};
          }
      }
    }
    __syncthreads();
    constexpr int CPR = OUTW / 8;
    #pragma unroll
    for (int i = 0; i < (BM * CPR) / (64 * NW); ++i) {
      const unsigned idx = i * (64u * NW) + tid, rl = idx / CPR, ch = idx % CPR;
      u32x4 v = *(const u32x4*)(lds + rl * RSTR + ch * 16u);
      const size_t go = (size_t)(m0 + rl) * LDC + n0 + ch * 8u;
      if constexpr (EPI == EPI_DOWN) {
        const u32x4 r = rres[i];
        #pragma unroll
        for (int e = 0; e < 4; ++e) {
          const float o0 = bflo(r[e]) + rbf(rgb[e >> 1][(2 * e) & 3] * bflo(v[e])), o1 = bfhi(r[e]) + rbf(rgb[e >> 1][(2 * e + 1) & 3] * bfhi(v[e]));
          v[e] = cvtpk(o0, o1);
        }
      }
      st16_wt((u16*)C + go, v);
    }
  }
}

constexpr size_t WS_MOD = 0, WS_H1 = 16384, WS_PART = WS_H1 + (size_t)SEQ * DM * 2,
                 WS_X1 = WS_PART + (size_t)HKV * SEQ * DM * 4, WS_H2 = WS_X1 + (size_t)SEQ * DM * 2, WS_HM = WS_H2 + (size_t)SEQ * DM * 2, WS_XN = WS_HM + (size_t)SEQ * DFF * 2, WS_END = WS_XN + (size_t)SEQ * DM * 2, WS_O = WS_END;

#define K_GU    gemm_kernel<128, 256, 2, 4, 3, EPI_GEGLU, DM,      DFF,     DFF,     8,  16, 14>
#define NST_DN 6
#define K_DOWN  gemm_kernel<64,  64,  2, 2, NST_DN, EPI_DOWN,  DFF,     DM,      DM,      28, 32, 8>

extern "C" void kernel_launch(void* const* d_in, const int* in_sizes, int n_in, void* d_out, int out_size, void* d_ws, size_t ws_size, hipStream_t stream) {
  const u16* x = (const u16*)d_in[0]; const u16* kc = (const u16*)d_in[1]; const u16* vc = (const u16*)d_in[2];
  const u16* wq = (const u16*)d_in[3]; const u16* wo = (const u16*)d_in[4];
  const u16* wg = (const u16*)d_in[6]; const u16* wu = (const u16*)d_in[7]; const u16* wd = (const u16*)d_in[8];
  const u16* cc = (const u16*)d_in[9]; const u16* wmod = (const u16*)d_in[10];
  char* ws = (char*)d_ws;
  float* mod = (float*)(ws + WS_MOD); u16* h1 = (u16*)(ws + WS_H1);
  float* part = (float*)(ws + WS_PART); u16* x1 = (u16*)(ws + WS_X1); u16* h2 = (u16*)(ws + WS_H2); u16* hm = (u16*)(ws + WS_HM);
  constexpr int SH_GU = 3 * (128 + 256) * 128, SH_DN = NST_DN * (64 + 64) * 128;
  (void)hipFuncSetAttribute((const void*)attn_fwd, hipFuncAttributeMaxDynamicSharedMemorySize, (int)SHM_ATTN);
  (void)hipFuncSetAttribute((const void*)K_GU, hipFuncAttributeMaxDynamicSharedMemorySize, SH_GU);
  (void)hipFuncSetAttribute((const void*)K_DOWN, hipFuncAttributeMaxDynamicSharedMemorySize, SH_DN);
  u16* xn = (u16*)(ws + WS_XN);
  pre_kernel<<<96 + SEQ / 4, 256, 0, stream>>>(cc, wmod, mod, x, xn);
  attn_fwd<<<256, 256, SHM_ATTN, stream>>>(xn, mod, wq, kc, vc, part, wo, wg, wu, wd, x, x1);
  K_GU<<<224, 512, SH_GU, stream>>>(h2, wg, wu, hm, nullptr, nullptr);
  K_DOWN<<<256, 256, SH_DN, stream>>>(hm, wd, wd, d_out, x1, mod);
}
```
